```python
import torch

def _softmax(x, axis=-1):
    return torch.softmax(x, dim=axis)
def _gelu(x):
    return torch.nn.functional.gelu(x, approximate="tanh")
def _silu(x):
    return torch.nn.functional.silu(x)
def _sigmoid(x):
    return torch.sigmoid(x)
def _layernorm(x, axis=-1, eps=1e-5):
    m = x.mean(dim=axis, keepdim=True)
    v = x.var(dim=axis, keepdim=True, unbiased=False)
    return (x - m) / torch.sqrt(v + eps)
def _scatter_add(tgt, idx, src, axis=0):
    return tgt.clone().index_add_(axis, idx, src)
def _segment_sum(src, seg, n):
    out = torch.zeros((n,) + src.shape[1:], dtype=src.dtype)
    return out.index_add_(0, seg, src)
def _pad(x, pw):
    flat = tuple(v for pair in reversed(pw) for v in pair)
    return torch.nn.functional.pad(x, flat)

kernel_name = 'gated_mix__softmax_full_mha__ssm_conv_s4__prompt'

def setup_inputs():

    _w = (lambda a: (lambda t: t.to(torch.bfloat16) if t.is_floating_point() else t)(torch.as_tensor(a)))
    def _A_setup():
        B, S, D = 1, 2048, 512
        return {
            "x": _w((lambda *s: torch.randn(*s, dtype=torch.bfloat16) * 0.05)(B, S, D)),
            "wqkv": _w((lambda *s: torch.randn(*s, dtype=torch.bfloat16) * 0.8)(D, 3 * D)),
            "wo": _w((lambda *s: torch.randn(*s, dtype=torch.bfloat16) * 0.05)(D, D)),
        }
    _A_in = _A_setup()

    def _B_setup():
        B, L, D, N = 1, 2048, 512, 128
        A_re = -(lambda _x: torch.exp(torch.as_tensor(_x)))((lambda *s: torch.randn(*s, dtype=torch.bfloat16) * 6.4)(D, N))
        A_im = torch.pi * (lambda *s: torch.randn(*s, dtype=torch.bfloat16) * 0.1)(D, N)
        B_re = (lambda *s: torch.randn(*s, dtype=torch.bfloat16) * 0.2)(D, N)
        B_im = (lambda *s: torch.randn(*s, dtype=torch.bfloat16) * 0.2)(D, N)
        C_re = (lambda *s: torch.randn(*s, dtype=torch.bfloat16) * 0.2)(D, N)
        C_im = (lambda *s: torch.randn(*s, dtype=torch.bfloat16) * 0.2)(D, N)
        log_dt = (lambda *s: torch.randn(*s, dtype=torch.bfloat16) * 0.1)(D) * 0.5 - 4.0
        Dskip = (lambda *s: torch.randn(*s, dtype=torch.bfloat16) * 0.1)(D)
        return {
            "u": _w((lambda *s: torch.randn(*s, dtype=torch.bfloat16) * 0.05)(B, L, D)),
            "A_re": _w(A_re),
            "A_im": _w(A_im),
            "B_re": _w(B_re),
            "B_im": _w(B_im),
            "C_re": _w(C_re),
            "C_im": _w(C_im),
            "log_dt": _w(log_dt),
            "Dskip": _w(Dskip),
        }
    _B_in = _B_setup()

    return {
        "A_x": _A_in["x"],
        "A_wqkv": _A_in["wqkv"],
        "A_wo": _A_in["wo"],
        "B_u": _B_in["u"],
        "B_A_re": _B_in["A_re"],
        "B_A_im": _B_in["A_im"],
        "B_B_re": _B_in["B_re"],
        "B_B_im": _B_in["B_im"],
        "B_C_re": _B_in["C_re"],
        "B_C_im": _B_in["C_im"],
        "B_log_dt": _B_in["log_dt"],
        "B_Dskip": _B_in["Dskip"],
        "gate_w": _w((lambda *s: torch.randn(*s, dtype=torch.bfloat16) * 0.05)(512) * 16.0),
    }

def reference(A_x, A_wqkv, A_wo, B_u, B_A_re, B_A_im, B_B_re, B_B_im, B_C_re, B_C_im, B_log_dt, B_Dskip, gate_w):
    def _A_ref(x, wqkv, wo):
        B, S, D = x.shape
        H = 32; Dh = D // H
        qkv = (lambda x, *d: x.permute(*d))((x @ wqkv).reshape(B, S, 3, H, Dh), 2, 0, 3, 1, 4)
        q, k, v = qkv[0], qkv[1], qkv[2]
        scale = Dh ** -0.5
        scores = torch.einsum("bhqd,bhkd->bhqk", q, k) * scale
        p = _softmax(scores, axis=-1)
        o = torch.einsum("bhqk,bhkd->bhqd", p, v)
        return (lambda x, *d: x.permute(*d))(o, 0, 2, 1, 3).reshape(B, S, D) @ wo

    def _B_ref(u, A_re, A_im, B_re, B_im, C_re, C_im, log_dt, Dskip):
        Bsz, L, D = u.shape
        dt = torch.exp(log_dt)[:, None]
        A = A_re + 1j * A_im
        Bc = B_re + 1j * B_im
        Cc = C_re + 1j * C_im
        logdA = dt * A
        dB = dt * Bc
        l = (lambda x, dt: x.to(dt))(torch.arange(L), A_re.dtype)
        P = torch.exp(logdA[:, :, None] * l[None, None, :])
        K = torch.einsum("dn,dnl,dn->dl", Cc, P, dB).real
        L2 = 2 * L
        Kp = _pad(K, ((0, 0), (0, L)))
        up = (lambda x, *d: x.permute(*d))(u, 0, 2, 1)
        up = _pad(up, ((0, 0), (0, 0), (0, L)))
        Kf = (lambda x, axes=(-2,-1): torch.fft.rfft2(x.float(), dim=axes))(Kp[:, None, :], axes=(-2, -1))
        Uf = (lambda x, axes=(-2,-1): torch.fft.rfft2(x.float(), dim=axes))(up[:, :, None, :], axes=(-2, -1))
        Yf = Uf * Kf[None, :, :, :]
        y = (lambda x, s, axes=(-2,-1): torch.fft.irfft2(x, s=s, dim=axes).to(torch.bfloat16))(Yf, (1, L2), axes=(-2, -1))[:, :, 0, :L]
        y = (lambda x, *d: x.permute(*d))(y, 0, 2, 1) + u * Dskip[None, None, :]
        return y

    x = A_x
    g = _sigmoid((x * gate_w).sum(-1, keepdims=True))
    a = _A_ref(x, A_wqkv, A_wo)
    b = _B_ref(x, B_A_re, B_A_im, B_B_re, B_B_im, B_C_re, B_C_im, B_log_dt, B_Dskip)
    return g * a + (1 - g) * b
```

```cpp
#include <hip/hip_runtime.h>
#include <hip/hip_bf16.h>
#include <stdint.h>

typedef unsigned short u16;
typedef __attribute__((ext_vector_type(16))) float f32x16;
typedef __attribute__((ext_vector_type(8))) float f32x8;
typedef __attribute__((ext_vector_type(4))) float f32x4;
typedef __attribute__((ext_vector_type(2))) float f32x2;
typedef __attribute__((ext_vector_type(2))) __bf16 bf16x2;
typedef __attribute__((ext_vector_type(8))) __bf16 bf16x8;
typedef __attribute__((ext_vector_type(16))) __bf16 bf16x16;
typedef __attribute__((ext_vector_type(4))) short s16x4;
typedef __attribute__((ext_vector_type(4))) unsigned u32x4;
typedef __attribute__((ext_vector_type(2))) unsigned u32x2;
#define LDS __attribute__((address_space(3)))
#define DEV __device__ __forceinline__

constexpr size_t WS_QF = 0;
constexpr size_t WS_KF = 2u << 20;
constexpr size_t WS_VF = 4u << 20;
constexpr size_t WS_OF = 6u << 20;
constexpr size_t WS_XT = 8u << 20;
constexpr size_t WS_YT = 10u << 20;
constexpr size_t WS_G  = 12u << 20;
constexpr size_t WS_KR = 13u << 20;
constexpr int KR_STRIDE = 2080;

DEV unsigned pack_bf16(float lo, float hi) {
  f32x2 a = {lo, hi};
  return __builtin_bit_cast(unsigned, __builtin_convertvector(a, bf16x2));
}
DEV float bf_lo(unsigned u) { return __builtin_bit_cast(float, u << 16); }
DEV float bf_hi(unsigned u) { return __builtin_bit_cast(float, u & 0xffff0000u); }
DEV float bf2f(u16 v) { return __builtin_bit_cast(float, (unsigned)v << 16); }
DEV float round_bf16(float f) { return bf_lo(pack_bf16(f, f)); }

DEV void dma16(unsigned lds_addr, unsigned voff, const void* sbase) {
  asm volatile("s_mov_b32 m0, %0\n\ts_nop 0\n\tglobal_load_lds_dwordx4 %1, %2" ::"s"(lds_addr), "v"(voff), "s"(sbase) : "memory");
}
#define WAIT_VM(n) asm volatile("s_waitcnt vmcnt(" #n ")" ::: "memory")
#define WAIT_LGKM0() asm volatile("s_waitcnt lgkmcnt(0)" ::: "memory")
#define CBAR() asm volatile("" ::: "memory")
DEV u32x4 lds_r128(unsigned a) { return *(LDS u32x4*)(size_t)a; }
DEV void lds_w128(unsigned a, u32x4 v) { *(LDS u32x4*)(size_t)a = v; }
DEV void lds_w32(unsigned a, unsigned v) { *(LDS unsigned*)(size_t)a = v; }
DEV float lds_rf32(unsigned a) { return *(LDS float*)(size_t)a; }
DEV u32x2 lds_tr(unsigned a) { return __builtin_bit_cast(u32x2, __builtin_amdgcn_ds_read_tr16_b64_v4i16((LDS s16x4*)(size_t)a)); }
DEV bf16x8 as_bf8(u32x4 v) { return __builtin_bit_cast(bf16x8, v); }
DEV f32x16 mfma32(u32x4 a, u32x4 b, f32x16 c) { return __builtin_amdgcn_mfma_f32_32x32x16_bf16(as_bf8(a), as_bf8(b), c, 0, 0, 0); }
DEV f32x4 mfma16(u32x4 a, u32x4 b, f32x4 c) { return __builtin_amdgcn_mfma_f32_16x16x32_bf16(as_bf8(a), as_bf8(b), c, 0, 0, 0); }
DEV void lds_flag_set(unsigned a) { asm volatile("ds_write_b32 %0, %1" ::"v"(a), "v"(1u) : "memory"); }
DEV void lds_flag_wait4(unsigned a) {
  for (;;) {
    u32x4 v;
    asm volatile("ds_read_b128 %0, %1\n\ts_waitcnt lgkmcnt(0)" : "=&v"(v) : "v"(a) : "memory");
    if (__builtin_amdgcn_readfirstlane(v[0] & v[1] & v[2] & v[3])) break;
  }
}
DEV void lds_flag_wait1(unsigned a) {
  for (;;) {
    unsigned v;
    asm volatile("ds_read_b32 %0, %1\n\ts_waitcnt lgkmcnt(0)" : "=&v"(v) : "v"(a) : "memory");
    if (__builtin_amdgcn_readfirstlane(v)) break;
  }
}

constexpr unsigned K1_W = 0;
constexpr unsigned K1_X = 73728;
constexpr unsigned K1_GEN = 139264;
constexpr unsigned K1_T = 156672;
constexpr unsigned K1_FLAG = 161856;
constexpr unsigned K1_LDS = 161952;

struct cf { float r, i; };
DEV cf cmul(cf a, cf b) { return cf{__builtin_fmaf(a.r, b.r, -a.i * b.i), __builtin_fmaf(a.r, b.i, a.i * b.r)}; }
DEV cf cseed(float a2, float br, float L) {
  float e = __builtin_amdgcn_exp2f(a2 * L);
  float ph = __builtin_amdgcn_fractf(br * L);
  return cf{e * __builtin_amdgcn_cosf(ph), e * __builtin_amdgcn_sinf(ph)};
}

template <int NV>
DEV void k1_gemm(const char* x, const char* wq, char* ws, int tb, int c, int w, int lane) {
  const int t0 = 128 * tb + 32 * w;
  const int r = lane & 31, h = lane >> 5;
  unsigned vx[2], vw[3];
#pragma unroll
  for (int p = 0; p < 2; ++p) vx[p] = (lane >> 3) * 1024 + (((lane & 7) ^ (4 * p + (lane >> 4))) << 4);
#pragma unroll
  for (int p = 0; p < 3; ++p) { int m = 64 * p + lane; int row = m / 12; vw[p] = row * 3072 + (m - 12 * row) * 16; }
  const char* xb = x + (size_t)t0 * 1024;
  const char* wb = wq + 192 * c + (size_t)(32 * w) * 3072;
  const unsigned xr = K1_X + w * 16384;
  auto issue_x = [&](int s) {
#pragma unroll
    for (int i = 0; i < 4; ++i) dma16(xr + (s & 3) * 4096 + i * 1024, vx[i & 1], xb + i * 8192 + s * 128);
  };
  auto issue_w = [&](int q) {
#pragma unroll
    for (int i = 0; i < 6; ++i)
      dma16(K1_W + (q % 3) * 24576 + w * 6144 + i * 1024, vw[i % 3], wb + (size_t)(128 * q + 16 * (i / 3)) * 3072);
  };
  issue_w(0); issue_x(0); issue_x(1); issue_w(1); issue_x(2); issue_x(3); issue_w(2);
  WAIT_LGKM0();
  __builtin_amdgcn_s_barrier();

  unsigned xa[4];
#pragma unroll
  for (int ks = 0; ks < 4; ++ks) xa[ks] = xr + r * 128 + ((((h ^ (r >> 1)) & 7) << 4) ^ (ks << 5));
  const int g = lane >> 4, q4 = (lane & 15) >> 2, pp = lane & 3;
  const unsigned wa0 = K1_W + (8 * (g >> 1) + q4) * 192 + (g & 1) * 32 + pp * 8;
  unsigned wa2 = wa0 + 49152;
  asm volatile("" : "+v"(wa2));
  f32x16 acc[3];
#pragma unroll
  for (int p = 0; p < 3; ++p)
#pragma unroll
    for (int i = 0; i < 16; ++i) acc[p][i] = 0.f;
  u32x2 xt[4];
#pragma unroll
  for (int m = 0; m < 4; ++m) xt[m] = u32x2{0, 0};
  const int sx = c >> 1;

#pragma unroll
  for (int s = 0; s < 8; ++s) {
    if (s == 0 || s == 1 || s == 3) WAIT_VM(24);
    else if (s == 2 || s == 4) WAIT_VM(18);
    else if (s == 5) WAIT_VM(14);
    else if (s == 6) WAIT_VM(4);
    else WAIT_VM(0);
    if ((s & 1) == 0) {
      lds_flag_set(K1_FLAG + (s >> 1) * 16 + w * 4);
      lds_flag_wait4(K1_FLAG + (s >> 1) * 16);
      if (s == 2) issue_w(3);
    }
    const int slot = (s >> 1) % 3;
    __builtin_amdgcn_sched_barrier(0);
#pragma unroll
    for (int ks = 0; ks < 4; ++ks) {
      u32x4 xf = lds_r128(xa[ks] + (s & 3) * 4096);
      const int kq = (s & 1) * 4 + ks;
#pragma unroll
      for (int p = 0; p < 3; ++p) {
        const unsigned a = (slot == 2 ? wa2 : wa0 + slot * 24576) + kq * 3072 + p * 64;
        u32x2 lo = lds_tr(a), hi = lds_tr(a + 768);
        u32x4 wf = {lo[0], lo[1], hi[0], hi[1]};
        if (p >= 3 - NV) acc[p] = mfma32(xf, wf, acc[p]);
        else acc[p] = mfma32(wf, xf, acc[p]);
      }
    }
    if (s == sx) {
#pragma unroll
      for (int m = 0; m < 4; ++m) {
        const int row = 16 * (g >> 1) + 4 * m + q4;
        const int ch = (4 * (c & 1) + 2 * (g & 1) + (pp >> 1)) ^ ((row >> 1) & 7);
        xt[m] = lds_tr(xr + (s & 3) * 4096 + row * 128 + ch * 16 + (pp & 1) * 8);
      }
    }
    asm volatile("" : "+v"(acc[0]), "+v"(acc[1]), "+v"(acc[2]));
    __builtin_amdgcn_sched_barrier(0);
    if (s < 4) { WAIT_LGKM0(); issue_x(s + 4); }
  }
  const int T = 4 * tb + w;
#pragma unroll
  for (int p = 0; p < 3; ++p) {
    const int G = 3 * c + p, hp = G & 15;
    if (p >= 3 - NV) {
      char* dst = ws + WS_VF + (size_t)((2 * hp + (r >> 4)) * 64 + T) * 1024 + h * 256 + (r & 15) * 16;
#pragma unroll
      for (int s = 0; s < 2; ++s) {
        u32x4 v;
#pragma unroll
        for (int j = 0; j < 4; ++j) v[j] = pack_bf16(acc[p][8 * s + 2 * j], acc[p][8 * s + 2 * j + 1]);
        *(u32x4*)(dst + s * 512) = v;
      }
    } else {
      const bool isq = G < 16;
      const float sc = isq ? 0.36067376022224085f : 1.0f;
      const int slotr = isq ? r : ((r & 19) | ((r & 4) << 1) | ((r & 8) >> 1));
      char* dst = ws + (isq ? WS_QF : WS_KF) + (size_t)(2 * hp * 64 + T) * 1024 + h * 512 + slotr * 16;
#pragma unroll
      for (int s = 0; s < 2; ++s) {
        u32x4 v;
#pragma unroll
        for (int j = 0; j < 4; ++j) v[j] = pack_bf16(acc[p][8 * s + 2 * j] * sc, acc[p][8 * s + 2 * j + 1] * sc);
        *(u32x4*)(dst + s * 65536) = v;
      }
    }
  }
  {
    char* dst = ws + WS_XT + (size_t)(32 * c + 16 * (g & 1) + (lane & 15)) * 4096 + (t0 + 16 * (g >> 1)) * 2;
    *(u32x4*)(dst) = u32x4{xt[0][0], xt[0][1], xt[1][0], xt[1][1]};
    *(u32x4*)(dst + 16) = u32x4{xt[2][0], xt[2][1], xt[3][0], xt[3][1]};
  }
}

DEV void k1_gen(const char* x, const u16* Are, const u16* Aim, const u16* Bre, const u16* Bim, const u16* Cre, const u16* Cim,
                const u16* logdt, const char* gatew, char* ws, int bid, int gw, int lane) {
  const int d = 2 * bid + (gw >> 1), hw = gw & 1;
  const int pi = d * 128 + 64 * hw + lane;
  const u16 rAre = Are[pi], rAim = Aim[pi], rBre = Bre[pi], rBim = Bim[pi], rCre = Cre[pi], rCim = Cim[pi], rdt = logdt[d];
  const int grow = 8 * bid + 2 * gw;
  const u32x4 gx0 = *(const u32x4*)(x + (size_t)grow * 1024 + lane * 16);
  const u32x4 gx1 = *(const u32x4*)(x + (size_t)grow * 1024 + 1024 + lane * 16);
  const u32x4 gwv = *(const u32x4*)(gatew + lane * 16);
  const unsigned tb = K1_T + (gw >> 1) * 2592;
  {
    const unsigned z = K1_T + gw * 1344 + lane * 16;
    lds_w128(z, u32x4{0, 0, 0, 0});
    if (lane < 20 && z + 1024 < K1_LDS) lds_w128(z + 1024, u32x4{0, 0, 0, 0});
  }
  WAIT_LGKM0();
  __builtin_amdgcn_s_barrier();
  unsigned ea[16];
#pragma unroll
  for (int it = 0; it < 2; ++it)
#pragma unroll
    for (int e = 0; e < 8; e += 2) {
      const int l0 = 2047 - (1024 * hw + 512 * it + 8 * lane) - e;
      const unsigned pr = pack_bf16((float)l0, (float)(l0 - 1));
      const int i0 = l0 < 128 ? l0 : (int)(pr & 0xffffu) - 0x4280;
      const int i1 = l0 - 1 < 128 ? l0 - 1 : (int)(pr >> 16) - 0x4280;
      ea[it * 8 + e] = tb + i0 * 4;
      ea[it * 8 + e + 1] = tb + i1 * 4;
    }
  {
    float s0 = 0.f, s1 = 0.f;
#pragma unroll
    for (int j = 0; j < 4; ++j) {
      const float wl = bf_lo(gwv[j]), wh = bf_hi(gwv[j]);
      s0 = __builtin_fmaf(bf_lo(gx0[j]), wl, s0); s0 = __builtin_fmaf(bf_hi(gx0[j]), wh, s0);
      s1 = __builtin_fmaf(bf_lo(gx1[j]), wl, s1); s1 = __builtin_fmaf(bf_hi(gx1[j]), wh, s1);
    }
#pragma unroll
    for (int o = 1; o < 64; o <<= 1) { s0 += __shfl_xor(s0, o); s1 += __shfl_xor(s1, o); }
    if (lane < 2) {
      const float z = round_bf16(lane ? s1 : s0);
      ((float*)(ws + WS_G))[grow + lane] = round_bf16(1.0f / (1.0f + __expf(-z)));
    }
  }
  const float dt = round_bf16(expf(bf2f(rdt)));
  const float a2 = (dt * bf2f(rAre)) * 1.4426950408889634f;
  const float br = (dt * bf2f(rAim)) * 0.15915494309189535f;
  const cf cc = cmul(cf{bf2f(rCre), bf2f(rCim)}, cf{dt * bf2f(rBre), dt * bf2f(rBim)});
  const cf w1 = cseed(a2, br, 1.f), w8 = cseed(a2, br, 8.f), W = cseed(a2, br, 128.f);
  const unsigned reg = K1_GEN + gw * 4352;
  const unsigned wa = reg + lane * 4;
  const unsigned ra = reg + (lane & 15) * 272 + (lane >> 4) * 16;
#define PUT(row, v) lds_w32(wa + (row) * 272, pack_bf16((v).r, (v).i))
  {
    cf R = cc;
#pragma unroll
    for (int j = 0; j < 16; ++j) { lds_w32(wa + j * 272, pack_bf16(R.r, -R.i)); if (j < 15) R = cmul(R, w8); }
  }
  CBAR();
  u32x4 bfr[4];
#pragma unroll
  for (int ks = 0; ks < 4; ++ks) bfr[ks] = lds_r128(ra + ks * 64);
  CBAR();
  cf p[8];
  p[0] = cf{1.f, 0.f}; p[1] = w1;
#pragma unroll
  for (int i = 2; i < 8; ++i) p[i] = cmul(p[i - 1], w1);
#pragma unroll
  for (int i = 0; i < 8; ++i) PUT(i, p[i]);
  PUT(8, W);
#pragma unroll
  for (int i = 1; i < 8; ++i) { cf t = cmul(W, p[i]); PUT(8 + i, t); }
  CBAR();
  f32x4 d0 = {0, 0, 0, 0}, d1 = {0, 0, 0, 0}, d2 = {0, 0, 0, 0};
#pragma unroll
  for (int ks = 0; ks < 4; ++ks) d0 = mfma16(lds_r128(ra + ks * 64), bfr[ks], d0);
  CBAR();
  cf Wp = cmul(W, W);
#pragma unroll
  for (int a = 0; a < 2; ++a) {
    PUT(4 * a, Wp);
#pragma unroll
    for (int i = 1; i < 4; ++i) { cf t = cmul(Wp, p[2 * i]); PUT(4 * a + i, t); }
    Wp = cmul(Wp, W);
  }
#pragma unroll
  for (int a = 0; a < 4; ++a) { PUT(8 + 2 * a, Wp); cf t = cmul(Wp, p[4]); PUT(9 + 2 * a, t); Wp = cmul(Wp, W); }
  CBAR();
#pragma unroll
  for (int ks = 0; ks < 4; ++ks) d1 = mfma16(lds_r128(ra + ks * 64), bfr[ks], d1);
  CBAR();
#pragma unroll
  for (int a = 0; a < 9; ++a) { PUT(a, Wp); if (a < 8) Wp = cmul(Wp, W); }
  CBAR();
#pragma unroll
  for (int ks = 0; ks < 4; ++ks) d2 = mfma16(lds_r128(ra + ks * 64), bfr[ks], d2);
#undef PUT
  {
    const int j = lane & 15, g = lane >> 4;
    LDS float* T = (LDS float*)(size_t)tb;
    const int i0 = 128 * (g >> 1) + 8 * j + 4 * (g & 1);
    const int i1a = g < 2 ? 256 + 64 * g + 4 * j : 384 + 64 * (g - 2) + 2 * j;
    const int i1b = g < 2 ? i1a + 2 : i1a + 32;
    const int i2 = 512 + 64 * g + j;
#pragma unroll
    for (int q = 0; q < 4; ++q) __hip_atomic_fetch_add(T + i0 + q, d0[q], __ATOMIC_RELAXED, __HIP_MEMORY_SCOPE_WORKGROUP);
    __hip_atomic_fetch_add(T + i1a, d1[0], __ATOMIC_RELAXED, __HIP_MEMORY_SCOPE_WORKGROUP);
    __hip_atomic_fetch_add(T + i1a + 1, d1[1], __ATOMIC_RELAXED, __HIP_MEMORY_SCOPE_WORKGROUP);
    __hip_atomic_fetch_add(T + i1b, d1[2], __ATOMIC_RELAXED, __HIP_MEMORY_SCOPE_WORKGROUP);
    __hip_atomic_fetch_add(T + i1b + 1, d1[3], __ATOMIC_RELAXED, __HIP_MEMORY_SCOPE_WORKGROUP);
    if (g < 2) {
#pragma unroll
      for (int q = 0; q < 4; ++q) __hip_atomic_fetch_add(T + i2 + 16 * q, d2[q], __ATOMIC_RELAXED, __HIP_MEMORY_SCOPE_WORKGROUP);
    } else if (lane == 32) {
      __hip_atomic_fetch_add(T + 640, d2[0], __ATOMIC_RELAXED, __HIP_MEMORY_SCOPE_WORKGROUP);
    }
  }
  WAIT_LGKM0();
  lds_flag_set(K1_FLAG + 64 + gw * 4);
  lds_flag_wait1(K1_FLAG + 64 + (gw ^ 1) * 4);
  char* kr = ws + WS_KR + (size_t)d * (KR_STRIDE * 2);
#pragma unroll
  for (int it = 0; it < 2; ++it) {
    u32x4 v;
#pragma unroll
    for (int e = 0; e < 4; ++e) v[e] = pack_bf16(lds_rf32(ea[it * 8 + 2 * e]), lds_rf32(ea[it * 8 + 2 * e + 1]));
    *(u32x4*)(kr + (1024 * hw + 512 * it + 8 * lane) * 2) = v;
  }
  if (hw == 1 && lane < 4) *(u32x4*)(kr + 4096 + lane * 16) = u32x4{0, 0, 0, 0};
}

__global__ __launch_bounds__(512, 2) void k1(const char* x, const char* wqkv, const u16* Are, const u16* Aim, const u16* Bre,
                                             const u16* Bim, const u16* Cre, const u16* Cim, const u16* logdt,
                                             const char* gatew, char* ws) {
  const int bid = blockIdx.x, lane = threadIdx.x & 63;
  const int w = __builtin_amdgcn_readfirstlane(threadIdx.x >> 6);
  if (w < 4) {
    const int xcd = bid & 7, j = bid >> 3;
    const int tb = 4 * (xcd >> 1) + (j & 3), c = 8 * (xcd & 1) + (j >> 2);
    if (c < 10) k1_gemm<0>(x, wqkv, ws, tb, c, w, lane);
    else if (c == 10) k1_gemm<1>(x, wqkv, ws, tb, c, w, lane);
    else k1_gemm<3>(x, wqkv, ws, tb, c, w, lane);
  } else {
    k1_gen(x, Are, Aim, Bre, Bim, Cre, Cim, logdt, gatew, ws, bid, w - 4, lane);
  }
}

constexpr unsigned K2_K = 0;
constexpr unsigned K2_V = 65536;
constexpr unsigned K2_U = 132096;
constexpr unsigned K2_LDS = 141312;

DEV f32x16 smfmac(u32x4 a, f32x8 p, f32x16 c, int idx) {
  return __builtin_amdgcn_smfmac_f32_32x32x32_bf16(as_bf8(a), __builtin_bit_cast(bf16x16, p), c, idx, 0, 0);
}
DEV float rowmax16(f32x16 s) {
  float m = s[0];
#pragma unroll
  for (int i = 1; i < 16; ++i) m = __builtin_fmaxf(m, s[i]);
  return m;
}
DEV float halfmax(float m) {
  auto r = __builtin_amdgcn_permlane32_swap(__builtin_bit_cast(unsigned, m), __builtin_bit_cast(unsigned, m), false, false);
  return __builtin_fmaxf(__builtin_bit_cast(float, r[0]), __builtin_bit_cast(float, r[1]));
}

DEV void k2_attn(char* ws, int head, int qb, int w, int lane) {
  const int r = lane & 31, h = lane >> 5;
  const int qt = qb * 8 + w;
  u32x4 qf;
  {
    const char* qp = ws + WS_QF + (size_t)(head * 64 + qt) * 1024;
    asm volatile("global_load_dwordx4 %0, %1, %2" : "=v"(qf) : "v"(lane * 16), "s"(qp) : "memory");
    const char* kp = ws + WS_KF + (size_t)(head * 64 + w) * 1024;
#pragma unroll
    for (int i = 0; i < 8; ++i) {
      dma16(K2_K + (8 * i + w) * 1024, lane * 16, kp + i * 8192);
      dma16(K2_V + (8 * i + w) * 1040, lane * 16, kp + (WS_VF - WS_KF) + i * 8192);
    }
  }
  if (w == 0) lds_w128(K2_V + lane * 1040 + 1024, u32x4{0x3f803f80u, 0x3f803f80u, 0x3f803f80u, 0x3f803f80u});
  asm volatile("s_waitcnt vmcnt(14) lgkmcnt(0)" : "+v"(qf)::"memory");
  __builtin_amdgcn_s_barrier();
  const unsigned ka = K2_K + lane * 16;
  const unsigned va0 = K2_V + (r < 16 ? h * 256 + r * 16 : 1024);
  const unsigned va1 = K2_V + (r < 16 ? 512 + h * 256 + r * 16 : 1024);
  const int idx = (int)0xDDDDDDDDu;
  f32x16 zero16;
#pragma unroll
  for (int i = 0; i < 16; ++i) zero16[i] = 0.f;
  float m = halfmax(rowmax16(mfma32(lds_r128(ka), qf, zero16)));
  f32x16 O;
  for (int pass = 0;; ++pass) {
    f32x16 negm;
#pragma unroll
    for (int i = 0; i < 16; ++i) negm[i] = -m;
    O = zero16;
    f32x16 S0 = mfma32(lds_r128(ka), qf, negm), S1;
    u32x4 kn = lds_r128(ka + 1024);
    u32x4 v0 = lds_r128(va0), v1 = lds_r128(va1);
    for (int tb = 0; tb < 64; tb += 8) {
      const unsigned kb = ka + tb * 1024, vb0 = va0 + tb * 1040, vb1 = va1 + tb * 1040;
#pragma unroll
      for (int t = 0; t < 8; t += 2) {
        if (t == 6 && tb == 0 && pass == 0) { WAIT_VM(0); __builtin_amdgcn_s_barrier(); }
        S1 = mfma32(kn, qf, negm);
        kn = lds_r128(kb + (t + 2) * 1024);
        u32x4 x0 = lds_r128(vb0 + (t + 1) * 1040), x1 = lds_r128(vb1 + (t + 1) * 1040);
        {
          f32x8 pa, pb;
#pragma unroll
          for (int i = 0; i < 8; ++i) pa[i] = __builtin_amdgcn_exp2f(S0[i]);
          O = smfmac(v0, pa, O, idx);
#pragma unroll
          for (int i = 0; i < 8; ++i) pb[i] = __builtin_amdgcn_exp2f(S0[8 + i]);
          O = smfmac(v1, pb, O, idx);
        }
        S0 = mfma32(kn, qf, negm);
        kn = lds_r128(kb + (t + 3) * 1024);
        v0 = lds_r128(vb0 + (t + 2) * 1040); v1 = lds_r128(vb1 + (t + 2) * 1040);
        {
          f32x8 pa, pb;
#pragma unroll
          for (int i = 0; i < 8; ++i) pa[i] = __builtin_amdgcn_exp2f(S1[i]);
          O = smfmac(x0, pa, O, idx);
#pragma unroll
          for (int i = 0; i < 8; ++i) pb[i] = __builtin_amdgcn_exp2f(S1[8 + i]);
          O = smfmac(x1, pb, O, idx);
        }
      }
    }
    if (pass == 1 || !__any(!(O[8] <= 1073741824.f))) break;
    float mm = -__builtin_inff();
    for (int t = 0; t < 64; ++t) mm = __builtin_fmaxf(mm, rowmax16(mfma32(lds_r128(ka + t * 1024), qf, zero16)));
    m = halfmax(mm);
  }
  const float inv = 1.0f / O[8];
  u32x4 ov;
#pragma unroll
  for (int j = 0; j < 4; ++j) ov[j] = pack_bf16(O[2 * j] * inv, O[2 * j + 1] * inv);
  *(u32x4*)(ws + WS_OF + (size_t)(qt * 32 + head) * 1024 + lane * 16) = ov;
}

typedef u32x4 __attribute__((aligned(2))) u32x4_u;
template <int C0, int C1, int C2, int C3>
DEV void k2_conv_tiles(const char* kr, char* yt, unsigned ub, int lane) {
  f32x4 a0 = {0, 0, 0, 0}, a1 = a0, a2 = a0, a3 = a0;
#pragma unroll
  for (int s = 0; s < 8 * (C3 + 1); ++s) {
    const u32x4 A = *(const u32x4_u*)(kr - 64 * s);
    if (s < 8 * (C0 + 1)) a0 = mfma16(A, lds_r128(ub + 512 * C0 + 4096 - 64 * s), a0);
    if (s < 8 * (C1 + 1)) a1 = mfma16(A, lds_r128(ub + 512 * C1 + 4096 - 64 * s), a1);
    if (s < 8 * (C2 + 1)) a2 = mfma16(A, lds_r128(ub + 512 * C2 + 4096 - 64 * s), a2);
    a3 = mfma16(A, lds_r128(ub + 512 * C3 + 4096 - 64 * s), a3);
  }
  *(u32x2*)(yt + 512 * C0) = u32x2{pack_bf16(a0[0], a0[1]), pack_bf16(a0[2], a0[3])};
  *(u32x2*)(yt + 512 * C1) = u32x2{pack_bf16(a1[0], a1[1]), pack_bf16(a1[2], a1[3])};
  *(u32x2*)(yt + 512 * C2) = u32x2{pack_bf16(a2[0], a2[1]), pack_bf16(a2[2], a2[3])};
  *(u32x2*)(yt + 512 * C3) = u32x2{pack_bf16(a3[0], a3[1]), pack_bf16(a3[2], a3[3])};
}
DEV void k2_conv(char* ws, int bid, int cw, int lane) {
  const int d = 2 * bid + (cw >> 1);
  const unsigned ur = K2_U + (cw >> 1) * 4608;
  __builtin_amdgcn_s_barrier();
  const char* xp = ws + WS_XT + (size_t)d * 4096;
#pragma unroll
  for (int i = 0; i < 4; ++i) dma16(ur + 512 + i * 1024, lane * 16, xp + i * 1024);
  if (lane < 32) lds_w128(ur + lane * 16, u32x4{0, 0, 0, 0});
  __builtin_amdgcn_s_barrier();
  asm volatile("s_waitcnt vmcnt(0) lgkmcnt(0)" ::: "memory");
  const int j = lane & 15, g = lane >> 4;
  const char* kr = ws + WS_KR + (size_t)d * (KR_STRIDE * 2) + (2031 - j + 8 * g) * 2;
  const unsigned ub = ur + 512 - 32 + 32 * j + 16 * g - 4096;
  char* yt = ws + WS_YT + (size_t)d * 4096 + (16 * j + 4 * g) * 2;
  if (cw & 1) k2_conv_tiles<2, 3, 4, 5>(kr, yt, ub, lane);
  else k2_conv_tiles<0, 1, 6, 7>(kr, yt, ub, lane);
}

__global__ __launch_bounds__(768, 3) void k2(char* ws) {
  const int bid = blockIdx.x, lane = threadIdx.x & 63;
  const int w = __builtin_amdgcn_readfirstlane(threadIdx.x >> 6);
  if (w < 8) k2_attn(ws, 4 * (bid & 7) + ((bid >> 3) & 3), bid >> 5, w, lane);
  else k2_conv(ws, bid, w - 8, lane);
}

constexpr unsigned K3_O = 0;
constexpr unsigned K3_W = 65536;
constexpr unsigned K3_LDS = 131072;

__global__ __launch_bounds__(256, 1) void k3(const char* x, const char* wo, const char* dskip, const char* ws, char* out) {
  const int bid = blockIdx.x, lane = threadIdx.x & 63;
  const int w = __builtin_amdgcn_readfirstlane(threadIdx.x >> 6);
  const int tt = 4 * (bid & 7) + ((bid >> 3) & 3), dt = bid >> 5;
  const unsigned vwo = (lane >> 3) * 1024 + (((lane & 7) ^ ((lane >> 2) & 4)) << 4);
  {
    const char* op = ws + WS_OF + (size_t)(2 * tt + (w & 1)) * 32768 + (size_t)(4 * (w >> 1)) * 1024;
    const char* wp = wo + 128 * dt + (size_t)(32 * w) * 1024;
#pragma unroll
    for (int gq = 0; gq < 4; ++gq)
#pragma unroll
      for (int i = 0; i < 4; ++i) {
        dma16(K3_O + (8 * gq + 4 * (w >> 1) + i) * 2048 + (w & 1) * 1024, lane * 16, op + (8 * gq + i) * 1024);
        dma16(K3_W + (128 * gq + 32 * w + 8 * i) * 128, vwo, wp + (size_t)(128 * gq + 8 * i) * 1024);
      }
  }
  const int tl = lane & 31, h = lane >> 5;
  const int g = lane >> 4, q4 = (lane & 15) >> 2, pp = lane & 3;
  const unsigned oa = K3_O + (w & 1) * 1024 + lane * 16;
  const unsigned wa = K3_W + (4 * (g >> 1) + q4) * 128 + (((4 * (w >> 1) + 2 * (g & 1) + (pp >> 1)) ^ ((q4 & 2) << 1)) << 4) + (pp & 1) * 8;
  f32x16 acc;
#pragma unroll
  for (int i = 0; i < 16; ++i) acc[i] = 0.f;
#pragma unroll
  for (int gq = 0; gq < 4; ++gq) {
    if (gq == 0) WAIT_VM(24); else if (gq == 1) WAIT_VM(16); else if (gq == 2) WAIT_VM(8); else WAIT_VM(0);
    __builtin_amdgcn_s_barrier();
    CBAR();
    __builtin_amdgcn_sched_barrier(0);
#pragma unroll
    for (int i = 0; i < 8; ++i) {
      const int hd = 8 * gq + i;
      u32x2 lo = lds_tr(wa + hd * 2048), hi = lds_tr(wa + hd * 2048 + 1024);
      acc = mfma32(u32x4{lo[0], lo[1], hi[0], hi[1]}, lds_r128(oa + hd * 2048), acc);
    }
    asm volatile("" : "+v"(acc));
    __builtin_amdgcn_sched_barrier(0);
  }
  const int t = 64 * tt + 32 * (w & 1) + tl;
  const int d0 = 64 * dt + 32 * (w >> 1) + 4 * h;
  const float gt = ((const float*)(ws + WS_G))[t];
  const float omg = 1.0f - gt;
  const u16* yt = (const u16*)(ws + WS_YT) + (size_t)d0 * 2048 + t;
#pragma unroll
  for (int m = 0; m < 4; ++m) {
    const u32x2 xv = *(const u32x2*)(x + (size_t)t * 1024 + (d0 + 8 * m) * 2);
    const u32x2 dv = *(const u32x2*)(dskip + (d0 + 8 * m) * 2);
    float o[4];
#pragma unroll
    for (int e = 0; e < 4; ++e) {
      const float xe = (e & 1) ? bf_hi(xv[e >> 1]) : bf_lo(xv[e >> 1]);
      const float de = (e & 1) ? bf_hi(dv[e >> 1]) : bf_lo(dv[e >> 1]);
      const float b = __builtin_fmaf(xe, de, bf2f(yt[(8 * m + e) * 2048]));
      o[e] = __builtin_fmaf(gt, acc[4 * m + e], omg * b);
    }
    *(u32x2*)(out + (size_t)t * 1024 + (d0 + 8 * m) * 2) = u32x2{pack_bf16(o[0], o[1]), pack_bf16(o[2], o[3])};
  }
}

extern "C" void kernel_launch(void* const* d_in, const int* in_sizes, int n_in, void* d_out, int out_size, void* d_ws,
                              size_t ws_size, hipStream_t stream) {
  (void)in_sizes; (void)n_in; (void)out_size; (void)ws_size;
  (void)hipFuncSetAttribute((const void*)k1, hipFuncAttributeMaxDynamicSharedMemorySize, K1_LDS);
  (void)hipFuncSetAttribute((const void*)k2, hipFuncAttributeMaxDynamicSharedMemorySize, K2_LDS);
  (void)hipFuncSetAttribute((const void*)k3, hipFuncAttributeMaxDynamicSharedMemorySize, K3_LDS);
  char* ws = (char*)d_ws;
  k1<<<256, 512, K1_LDS, stream>>>((const char*)d_in[0], (const char*)d_in[1], (const u16*)d_in[4], (const u16*)d_in[5],
                                   (const u16*)d_in[6], (const u16*)d_in[7], (const u16*)d_in[8], (const u16*)d_in[9],
                                   (const u16*)d_in[10], (const char*)d_in[12], ws);
  k2<<<256, 768, K2_LDS, stream>>>(ws);
  k3<<<256, 256, K3_LDS, stream>>>((const char*)d_in[0], (const char*)d_in[2], (const char*)d_in[11], ws, (char*)d_out);
}
```

```cpp
#include <hip/hip_runtime.h>
#include <hip/hip_bf16.h>
#include <stdint.h>

typedef unsigned short u16;
typedef unsigned long long u64;
typedef __attribute__((ext_vector_type(16))) float f32x16;
typedef __attribute__((ext_vector_type(8))) float f32x8;
typedef __attribute__((ext_vector_type(4))) float f32x4;
typedef __attribute__((ext_vector_type(2))) float f32x2;
typedef __attribute__((ext_vector_type(2))) __bf16 bf16x2;
typedef __attribute__((ext_vector_type(8))) __bf16 bf16x8;
typedef __attribute__((ext_vector_type(4))) short s16x4;
typedef __attribute__((ext_vector_type(4))) unsigned u32x4;
typedef __attribute__((ext_vector_type(2))) unsigned u32x2;
#define LDS __attribute__((address_space(3)))
#define DEV __device__ __forceinline__

constexpr unsigned WS_QF = 0;
constexpr unsigned WS_KF = 2u << 20;
constexpr unsigned WS_VF = 4u << 20;
constexpr unsigned WS_OF = 6u << 20;
constexpr unsigned WS_XT = 8u << 20;
constexpr unsigned WS_YT = 10u << 20;
constexpr unsigned WS_G  = 12u << 20;
constexpr unsigned WS_EP = (12u << 20) + 65536;
constexpr unsigned WS_FA = WS_EP + 4096;
constexpr unsigned WS_FY = WS_FA + 32768;
constexpr unsigned WS_FO = WS_FY + 32768;
constexpr unsigned WS_KR = 13u << 20;
constexpr int KR_STRIDE = 2080;
#define KR_COPIES 2
#define NKERN 1

constexpr unsigned A_W = 0;
constexpr unsigned A_X = 49152;
constexpr unsigned A_GEN = 128512;
constexpr unsigned KVS = 2064;
constexpr unsigned B_U = 132352;
constexpr unsigned C_ST = 132352;
constexpr unsigned C_O = 0;
constexpr unsigned FL = 163328;
constexpr unsigned F_LANDED = FL, F_DONE = FL + 64, F_CH = FL + 128, F_GP = FL + 144, F_GE = FL + 160, F_GD = FL + 176,
                   F_F0 = FL + 192, F_F1 = FL + 224, F_FS = FL + 256, F_CD = FL + 272, F_AD = FL + 288, F_OC = FL + 320,
                   F_TAGOK = FL + 352, F_TAG = FL + 368, F_KS = FL + 384, F_GO = FL + 400, F_PD = FL + 416;
constexpr unsigned LDS_BYTES = 163840;

#define PROF_DECL
#define STAMP(i)
#define STAMPN(i)
#define PROF_ARG
#define PROF_PASS
#define PROF_END(kid, ws_)

DEV unsigned pack_bf16(float lo, float hi) {
  f32x2 a = {lo, hi};
  return __builtin_bit_cast(unsigned, __builtin_convertvector(a, bf16x2));
}
DEV float u2f(unsigned u) { return __builtin_bit_cast(float, u); }
DEV unsigned f2u(float f) { return __builtin_bit_cast(unsigned, f); }
DEV float bf_lo(unsigned u) { return u2f(u << 16); }
DEV float bf_hi(unsigned u) { return u2f(u & 0xffff0000u); }
DEV float round_bf16(float f) { return bf_lo(pack_bf16(f, f)); }

#define WAIT_VM(n) asm volatile("s_waitcnt vmcnt(" #n ")" ::: "memory")
#define WAIT_LGKM0() asm volatile("s_waitcnt lgkmcnt(0)" ::: "memory")
#define CBAR() asm volatile("" ::: "memory")
#define SB() __builtin_amdgcn_sched_barrier(0)
#define PRIO(n) __builtin_amdgcn_s_setprio(n)
DEV void dma16(unsigned lds_addr, unsigned voff, const void* sbase) {
  asm volatile("s_mov_b32 m0, %0\n\ts_nop 0\n\tglobal_load_lds_dwordx4 %1, %2" ::"s"(lds_addr), "v"(voff), "s"(sbase) : "memory");
}
DEV void dma16_dev(unsigned lds_addr, unsigned voff, const void* sbase) {
  asm volatile("s_mov_b32 m0, %0\n\ts_nop 0\n\tglobal_load_lds_dwordx4 %1, %2 sc1" ::"s"(lds_addr), "v"(voff), "s"(sbase) : "memory");
}
DEV void dma16x4(unsigned lds_addr, const unsigned* v, const void* sbase) {
  asm volatile("s_mov_b32 m0, %0\n\ts_nop 0\n\tglobal_load_lds_dwordx4 %1, %5\n\tglobal_load_lds_dwordx4 %2, %5 offset:1024\n\t"
               "global_load_lds_dwordx4 %3, %5 offset:2048\n\tglobal_load_lds_dwordx4 %4, %5 offset:3072"
               ::"s"(lds_addr), "v"(v[0]), "v"(v[1]), "v"(v[2]), "v"(v[3]), "s"(sbase) : "memory");
}
DEV u32x4 lds_r128(unsigned a) { return *(LDS u32x4*)(size_t)a; }
DEV void lds_w128(unsigned a, u32x4 v) { *(LDS u32x4*)(size_t)a = v; }
DEV void lds_w32(unsigned a, unsigned v) { *(LDS unsigned*)(size_t)a = v; }
DEV float lds_rf32(unsigned a) { return *(LDS float*)(size_t)a; }
DEV u32x2 lds_tr(unsigned a) { return __builtin_bit_cast(u32x2, __builtin_amdgcn_ds_read_tr16_b64_v4i16((LDS s16x4*)(size_t)a)); }
DEV bf16x8 as_bf8(u32x4 v) { return __builtin_bit_cast(bf16x8, v); }
DEV f32x16 mfma32(u32x4 a, u32x4 b, f32x16 c) { return __builtin_amdgcn_mfma_f32_32x32x16_bf16(as_bf8(a), as_bf8(b), c, 0, 0, 0); }
DEV f32x4 mfma16(u32x4 a, u32x4 b, f32x4 c) { return __builtin_amdgcn_mfma_f32_16x16x32_bf16(as_bf8(a), as_bf8(b), c, 0, 0, 0); }
#define MFMA32(D, A, B) asm volatile("v_mfma_f32_32x32x16_bf16 %0, %1, %2, %0" : "+v"(D) : "v"(A), "v"(B))
DEV void st16_dev(const void* base, unsigned off, u32x4 v) { asm volatile("global_store_dwordx4 %0, %1, %2 sc1\n\ts_nop 1" ::"v"(off), "v"(v), "s"(base) : "memory"); }
DEV void st8_dev(const void* base, unsigned off, u32x2 v) { asm volatile("global_store_dwordx2 %0, %1, %2 sc1" ::"v"(off), "v"(v), "s"(base) : "memory"); }
DEV void st4_dev(const void* base, unsigned off, unsigned v) { asm volatile("global_store_dword %0, %1, %2 sc1" ::"v"(off), "v"(v), "s"(base) : "memory"); }

DEV void lds_flag_set(unsigned a) { asm volatile("ds_write_b32 %0, %1" ::"v"(a), "v"(1u) : "memory"); }
template <int NW, int SLEEP> DEV void lds_wait(unsigned a) {
#pragma clang loop unroll(disable)
  for (int n = 0; n < (1 << 21); ++n) {
    unsigned ok;
    if (NW == 8) {
      u32x4 v, u;
      asm volatile("ds_read_b128 %0, %2\n\tds_read_b128 %1, %2 offset:16\n\ts_waitcnt lgkmcnt(0)" : "=&v"(v), "=&v"(u) : "v"(a) : "memory");
      ok = v[0] & v[1] & v[2] & v[3] & u[0] & u[1] & u[2] & u[3];
    } else if (NW == 4) {
      u32x4 v;
      asm volatile("ds_read_b128 %0, %1\n\ts_waitcnt lgkmcnt(0)" : "=&v"(v) : "v"(a) : "memory");
      ok = v[0] & v[1] & v[2] & v[3];
    } else {
      asm volatile("ds_read_b32 %0, %1\n\ts_waitcnt lgkmcnt(0)" : "=&v"(ok) : "v"(a) : "memory");
    }
    if (__builtin_amdgcn_readfirstlane(ok)) break;
    if (SLEEP) __builtin_amdgcn_s_sleep(SLEEP);
  }
}
DEV u32x4 lds_peek4(unsigned a) { return *(volatile LDS u32x4*)(size_t)a; }
DEV u32x2 get_tag() {
  lds_wait<1, 1>(F_TAGOK);
  return *(volatile LDS u32x2*)(size_t)F_TAG;
}
#define PSLEEP 4
DEV void publish(char* ws, unsigned off, u32x2 tag, int lane) {
  WAIT_VM(0);
  if (lane == 0) st8_dev(ws, off, tag);
}
DEV void poll1(const char* ws, unsigned off, u32x2 tag) {
#pragma clang loop unroll(disable)
  for (int n = 0; n < (1 << 17); ++n) {
    u32x2 a;
    asm volatile("global_load_dwordx2 %0, %1, %2 sc1\n\ts_waitcnt vmcnt(0)" : "=&v"(a) : "v"(off), "s"(ws) : "memory");
    if (!__any(a[0] != tag[0] || a[1] != tag[1])) break;
    __builtin_amdgcn_s_sleep(PSLEEP);
  }
}
DEV void poll2(const char* ws, unsigned off0, unsigned off1, u32x2 tag) {
#pragma clang loop unroll(disable)
  for (int n = 0; n < (1 << 17); ++n) {
    u32x2 a, b;
    asm volatile("global_load_dwordx2 %0, %2, %4 sc1\n\tglobal_load_dwordx2 %1, %3, %4 sc1\n\ts_waitcnt vmcnt(0)"
                 : "=&v"(a), "=&v"(b) : "v"(off0), "v"(off1), "s"(ws) : "memory");
    if (!__any(a[0] != tag[0] || a[1] != tag[1] || b[0] != tag[0] || b[1] != tag[1])) break;
    __builtin_amdgcn_s_sleep(PSLEEP);
  }
}

#define STAG 6
DEV void a_load(const char* x, const char* wq, int tb, int c, int s0, int l, int lane PROF_ARG) {
  unsigned vw[12], vx[16];
#pragma unroll
  for (int n = 0; n < 12; ++n) {
    const int m = 64 * (n % 3) + lane, row = m / 12;
    vw[n] = row * 3072 + (m - 12 * row) * 16 + (n / 3) * 49152 + 3072 - (n & 3) * 1024;
  }
#pragma unroll
  for (int n = 0; n < 16; ++n)
    vx[n] = (lane >> 3) * 1024 + (((lane & 7) ^ (4 * (n & 1) + (lane >> 4))) << 4) + (n >> 2) * 32768 + (n & 3) * 7168;
  wq += 192 * c - 3072;
  x += (size_t)(128 * tb) * 1024;
  if (l > 0) {
    __builtin_amdgcn_s_barrier();
    for (int n = 1; n < l; ++n) __builtin_amdgcn_s_sleep(STAG);
  }
#pragma unroll
  for (int half = 0; half < 2; ++half) {
    const int s = (s0 + l + 4 * half) & 7;
    if (half) lds_wait<4, 0>(F_DONE + l * 16);
    STAMP(16 + 3 * half);
#pragma unroll
    for (int q = 0; q < 4; ++q) {
      if (q < 3) dma16x4(A_W + l * 12288 + q * 4096, vw + 4 * q, wq + (size_t)s * (64 * 3072));
      if (half == 0 && q == 2 && l == 0) __builtin_amdgcn_s_barrier();
      dma16x4(A_X + q * 16384 + l * 4096, vx + 4 * q, x + s * 128);
    }
    STAMP(17 + 3 * half);
    WAIT_VM(0);
    lds_flag_set(F_LANDED + (l + 4 * half) * 4);
    STAMP(18 + 3 * half);
  }
}

template <int NV>
DEV void a_gemm(char* ws, int tb, int c, int s0, int w, int lane PROF_ARG) {
  const int r = lane & 31, h = lane >> 5;
  const unsigned xr = A_X + w * 16384;
  unsigned xa[4];
#pragma unroll
  for (int ks = 0; ks < 4; ++ks) { xa[ks] = xr + r * 128 + ((((h ^ (r >> 1)) & 7) << 4) ^ (ks << 5)); asm volatile("" : "+v"(xa[ks])); }
  const int g = lane >> 4, q4 = (lane & 15) >> 2, pp = lane & 3;
  unsigned wa = A_W + (8 * (g >> 1) + q4) * 192 + (g & 1) * 32 + pp * 8;
  asm volatile("" : "+v"(wa));
  unsigned xta[4];
  const int ix = ((c >> 1) - s0) & 7;
#pragma unroll
  for (int m = 0; m < 4; ++m) {
    const int row = 8 * g + 4 * (m & 1) + q4;
    const int ch = (4 * (c & 1) + 2 * (m >> 1) + (pp >> 1)) ^ ((row >> 1) & 7);
    xta[m] = xr + (ix & 3) * 4096 + row * 128 + ch * 16 + (pp & 1) * 8;
  }
  const int T = 4 * tb + w;
  f32x16 acc[3];
#pragma unroll
  for (int p = 0; p < 3; ++p)
#pragma unroll
    for (int i = 0; i < 16; ++i) acc[p][i] = 0.f;
  asm volatile("" : "+v"(acc[0]), "+v"(acc[1]), "+v"(acc[2]), "+v"(xta[0]), "+v"(xta[1]), "+v"(xta[2]), "+v"(xta[3]));
  __builtin_amdgcn_s_barrier();
  const u32x2 tag = get_tag();
  lds_wait<1, 0>(F_LANDED);
  STAMP(1);
  constexpr int PF = 2, NB = PF + 1;
  u32x4 xf[NB];
  u32x2 wl[NB][3], wh[NB][3];
  auto rdx = [&](int k) { xf[k % NB] = lds_r128(xa[k & 3] + ((k >> 2) & 3) * 4096); };
  auto rdw = [&](int k, int p) {
    wl[k % NB][p] = lds_tr(wa + ((k >> 2) & 3) * 12288 + (k & 3) * 3072 + p * 64);
    wh[k % NB][p] = lds_tr(wa + ((k >> 2) & 3) * 12288 + (k & 3) * 3072 + p * 64 + 768);
  };
#pragma unroll
  for (int k = 0; k < PF; ++k) { rdx(k); rdw(k, 0); rdw(k, 1); rdw(k, 2); }
  unsigned pk = 0;
#pragma unroll
  for (int k = 0; k < 32; ++k) {
    const int i = k >> 2, ks = k & 3;
    if (ks == 0) STAMPN(16 + i);
    SB();
#pragma unroll
    for (int p = 0; p < 3; ++p) {
      const u32x4 wf = {wl[k % NB][p][0], wl[k % NB][p][1], wh[k % NB][p][0], wh[k % NB][p][1]};
      if (p >= 3 - NV) MFMA32(acc[p], xf[k % NB], wf); else MFMA32(acc[p], wf, xf[k % NB]);
      SB();
      if (ks == 0 && p == 0 && __builtin_expect(i == ix, 0)) {
        const u32x2 a = lds_tr(xta[0]), b = lds_tr(xta[1]), cc = lds_tr(xta[2]), dd = lds_tr(xta[3]);
        const unsigned off = WS_XT + (T * 512 + 32 * c + (lane & 15)) * 64 + g * 16;
        st16_dev(ws, off, u32x4{a[0], a[1], b[0], b[1]});
        st16_dev(ws, off + 1024, u32x4{cc[0], cc[1], dd[0], dd[1]});
      }
      if (ks == 4 - PF && p == 0 && i < 7) {
        if (__builtin_expect(!__builtin_amdgcn_readfirstlane(pk), 0)) lds_wait<1, 0>(F_LANDED + (i + 1) * 4);
        CBAR();
      }
      if (k + PF < 32) { if (p == 0) rdx(k + PF); rdw(k + PF, p); }
      if (ks == 3 - PF && p == 1 && i < 7) pk = *(volatile LDS unsigned*)(size_t)(F_LANDED + (i + 1) * 4);
      if (ks == 3 - PF && p == 2) {
        if (i < 4) lds_flag_set(F_DONE + i * 16 + w * 4);
        else if (i == 7) lds_flag_set(F_GD + w * 4);
      }
      SB();
    }
  }
  asm volatile("s_nop 15" : "+v"(acc[0]), "+v"(acc[1]), "+v"(acc[2]));
  STAMP(2);
#pragma unroll
  for (int p = 0; p < 3; ++p) {
    const int G = 3 * c + p, hp = G & 15;
    if (p >= 3 - NV) {
      const unsigned off = WS_VF + ((2 * hp + (r >> 4)) * 64 + T) * 1024 + h * 256 + (r & 15) * 16;
#pragma unroll
      for (int s = 0; s < 2; ++s) {
        u32x4 v;
#pragma unroll
        for (int j = 0; j < 4; ++j) v[j] = pack_bf16(acc[p][8 * s + 2 * j], acc[p][8 * s + 2 * j + 1]);
        st16_dev(ws, off + s * 512, v);
      }
    } else {
      const bool isq = G < 16;
      const float sc = isq ? 0.36067376022224085f : 1.0f;
      const int slotr = isq ? r : ((r & 19) | ((r & 4) << 1) | ((r & 8) >> 1));
      const unsigned off = (isq ? WS_QF : WS_KF) + (2 * hp * 64 + T) * 1024 + h * 512 + slotr * 16;
#pragma unroll
      for (int s = 0; s < 2; ++s) {
        u32x4 v;
#pragma unroll
        for (int j = 0; j < 4; ++j) v[j] = pack_bf16(acc[p][8 * s + 2 * j] * sc, acc[p][8 * s + 2 * j + 1] * sc);
        st16_dev(ws, off + s * 65536, v);
      }
    }
  }
  WAIT_VM(0);
  lds_flag_set(F_PD + w * 4);
  if (w == 0) {
    lds_wait<4, 0>(F_PD);
    if (lane == 0) st8_dev(ws, WS_FA + (c * 16 + tb) * 128, tag);
  }
}

struct cf { float r, i; };
DEV cf cmul(cf a, cf b) {
  cf o;
  asm("v_mul_f32 %0, %3, %5\n\tv_mul_f32 %1, %3, %4\n\tv_fma_f32 %0, %2, %4, -%0\n\tv_fmac_f32 %1, %2, %5"
      : "=&v"(o.r), "=&v"(o.i) : "v"(a.r), "v"(a.i), "v"(b.r), "v"(b.i));
  return o;
}
DEV void cmul2(cf& r0, cf a0, cf b0, cf& r1, cf a1, cf b1) {
  cf o, q;
  asm("v_mul_f32 %0, %5, %7\n\tv_mul_f32 %1, %5, %6\n\tv_mul_f32 %2, %9, %11\n\tv_mul_f32 %3, %9, %10\n\t"
      "v_fma_f32 %0, %4, %6, -%0\n\tv_fmac_f32 %1, %4, %7\n\tv_fma_f32 %2, %8, %10, -%2\n\tv_fmac_f32 %3, %8, %11"
      : "=&v"(o.r), "=&v"(o.i), "=&v"(q.r), "=&v"(q.i)
      : "v"(a0.r), "v"(a0.i), "v"(b0.r), "v"(b0.i), "v"(a1.r), "v"(a1.i), "v"(b1.r), "v"(b1.i));
  r0 = o; r1 = q;
}
DEV cf cseed(float a2, float br, float L) {
  const float e = __builtin_amdgcn_exp2f(a2 * L);
  const float ph = __builtin_amdgcn_fractf(br * L);
  return cf{e * __builtin_amdgcn_cosf(ph), e * __builtin_amdgcn_sinf(ph)};
}
template <int CTRL> DEV float dpp_add(float v) {
  return v + u2f(__builtin_amdgcn_update_dpp(0, f2u(v), CTRL, 0xf, 0xf, true));
}
struct Params {
  const char *x, *wqkv, *wo, *Are, *Aim, *Bre, *Bim, *Cre, *Cim, *logdt, *dskip, *gatew;
  char *ws, *out;
};

DEV u32x2 s_gen(const Params& P, int bid, int gw, int lane, u32x4 (&kv)[4] PROF_ARG) {
  char* ws = P.ws;
  const int d = 2 * bid + (gw >> 1), hw = gw & 1;
  const int pi = d * 128 + 64 * hw + lane;
  const int grow = 8 * bid + 2 * gw;
  unsigned rdt, rds, rAre, rAim, rBre, rBim, rCre, rCim;
  u32x4 gx0, gx1, gwv;
  u32x2 tag;
#define LDU16(dst, base, off) asm volatile("global_load_ushort %0, %1, %2" : "=v"(dst) : "v"(off), "s"(base) : "memory")
#define LD16(dst, base, off) asm volatile("global_load_dwordx4 %0, %1, %2" : "=v"(dst) : "v"(off), "s"(base) : "memory")
  asm volatile("global_load_dwordx2 %0, %1, %2 sc1" : "=v"(tag) : "v"(WS_EP), "s"(ws) : "memory");
  LDU16(rdt, P.logdt, d * 2); LDU16(rAre, P.Are, pi * 2); LDU16(rAim, P.Aim, pi * 2); LDU16(rBre, P.Bre, pi * 2);
  LDU16(rBim, P.Bim, pi * 2); LDU16(rCre, P.Cre, pi * 2); LDU16(rCim, P.Cim, pi * 2); LDU16(rds, P.dskip, d * 2);
  LD16(gx0, P.x, grow * 1024 + lane * 16); LD16(gx1, P.x, grow * 1024 + 1024 + lane * 16); LD16(gwv, P.gatew, lane * 16);
  if (gw == 0 && lane < 32) lds_w128(FL + lane * 16, u32x4{0, 0, 0, 0});
  WAIT_LGKM0();
  __builtin_amdgcn_s_barrier();
  PRIO(0);
  const unsigned reg = A_GEN + gw * 8704;
  unsigned ea[18];
#pragma unroll
  for (int it = 0; it < 2; ++it)
#pragma unroll
    for (int e = 0; e < 10; e += 2) {
      const int l0 = 2047 - (1024 * hw + 512 * it + 8 * lane) - e;
      const unsigned pr = pack_bf16((float)l0, (float)(l0 - 1));
      const int i0 = l0 < 128 ? (l0 < 0 ? 641 : l0) : (int)(pr & 0xffffu) - 0x4280;
      ea[it * 9 + e] = A_GEN + (gw & 2) * 8704 + i0 * 4;
      if (e < 8) ea[it * 9 + e + 1] = A_GEN + (gw & 2) * 8704 + (l0 - 1 < 128 ? l0 - 1 : (int)(pr >> 16) - 0x4280) * 4;
    }
#pragma unroll
  for (int i = 0; i < 18; ++i) asm volatile("" : "+v"(ea[i]));
  asm volatile("s_waitcnt vmcnt(0)" : "+v"(tag), "+v"(rdt), "+v"(rds), "+v"(rAre), "+v"(rAim), "+v"(rBre), "+v"(rBim), "+v"(rCre),
               "+v"(rCim), "+v"(gx0), "+v"(gx1), "+v"(gwv)::"memory");
  if (gw == 0) {
    tag[0] += 1;
    *(volatile LDS u32x2*)(size_t)F_TAG = tag;
    lds_flag_set(F_TAGOK);
  }
  STAMP(1);
  const float dt = round_bf16(expf(bf_lo(rdt)));
  const float a2 = (dt * bf_lo(rAre)) * 1.4426950408889634f;
  const float br = (dt * bf_lo(rAim)) * 0.15915494309189535f;
  const cf w1 = cseed(a2, br, 1.f), W = cseed(a2, br, 128.f);
  cf w8c = cseed(a2, br, 8.f); w8c.i = -w8c.i;
  cf R = cmul(cf{bf_lo(rCre), -bf_lo(rCim)}, cf{dt * bf_lo(rBre), -(dt * bf_lo(rBim))});
  const unsigned wa = reg + lane * 4;
  const unsigned ra = reg + (lane & 15) * 272 + (lane >> 4) * 16;
#define PUT(area, row, v) lds_w32(wa + (area) * 4352 + (row) * 272, pack_bf16((v).r, (v).i))
  cf p[8], Wp[17];
  p[0] = cf{1.f, 0.f}; p[1] = w1; Wp[1] = W;
  PUT(1, 0, p[0]); PUT(1, 1, p[1]); PUT(1, 8, W); PUT(0, 0, R);
#pragma unroll
  for (int i = 2; i < 8; ++i) { cmul2(p[i], p[i - 1], w1, R, R, w8c); PUT(1, i, p[i]); PUT(0, i - 1, R); }
#pragma unroll
  for (int i = 1; i < 8; ++i) { cf t; cmul2(t, W, p[i], R, R, w8c); PUT(1, 8 + i, t); PUT(0, 6 + i, R); }
  cmul2(Wp[2], W, W, R, R, w8c); PUT(0, 14, R);
  cmul2(Wp[3], Wp[2], W, R, R, w8c); PUT(0, 15, R);
  CBAR();
  u32x4 bfr[4], afr[4];
#pragma unroll
  for (int ks = 0; ks < 4; ++ks) { bfr[ks] = lds_r128(ra + ks * 64); afr[ks] = lds_r128(ra + 4352 + ks * 64); }
  CBAR();
  PUT(0, 0, Wp[2]); PUT(0, 4, Wp[3]);
#pragma unroll
  for (int i = 1; i < 4; ++i) { cf t, u; cmul2(t, Wp[2], p[2 * i], u, Wp[3], p[2 * i]); PUT(0, i, t); PUT(0, 4 + i, u); }
  cmul2(Wp[4], Wp[2], Wp[2], Wp[5], Wp[3], Wp[2]);
  cmul2(Wp[6], Wp[3], Wp[3], Wp[7], Wp[4], Wp[3]);
#pragma unroll
  for (int a = 0; a < 4; a += 2) {
    cf t, u; cmul2(t, Wp[4 + a], p[4], u, Wp[5 + a], p[4]);
    PUT(0, 8 + 2 * a, Wp[4 + a]); PUT(0, 9 + 2 * a, t); PUT(0, 10 + 2 * a, Wp[5 + a]); PUT(0, 11 + 2 * a, u);
  }
  f32x4 d0 = {0, 0, 0, 0}, d1 = d0, d2 = d0;
#pragma unroll
  for (int ks = 0; ks < 4; ++ks) d0 = mfma16(afr[ks], bfr[ks], d0);
#pragma unroll
  for (int a = 8; a < 16; a += 2) { cmul2(Wp[a], Wp[a - 4], Wp[4], Wp[a + 1], Wp[a - 3], Wp[4]); PUT(1, a - 8, Wp[a]); PUT(1, a - 7, Wp[a + 1]); }
  Wp[16] = cmul(Wp[8], Wp[8]); PUT(1, 8, Wp[16]);
  CBAR();
#pragma unroll
  for (int ks = 0; ks < 4; ++ks) { d1 = mfma16(lds_r128(ra + ks * 64), bfr[ks], d1); d2 = mfma16(lds_r128(ra + 4352 + ks * 64), bfr[ks], d2); }
#undef PUT
  CBAR();
  {
    const int j = lane & 15, g = lane >> 4;
    LDS float* T = (LDS float*)(size_t)reg;
    const int i1a = g < 2 ? 256 + 64 * g + 4 * j : 384 + 64 * (g - 2) + 2 * j;
    if (lane == 0 && hw == 0) d0[0] += bf_lo(rds);
    *(LDS f32x4*)(T + 128 * (g >> 1) + 8 * j + 4 * (g & 1)) = d0;
    *(LDS f32x2*)(T + i1a) = f32x2{d1[0], d1[1]};
    *(LDS f32x2*)(T + (g < 2 ? i1a + 2 : i1a + 32)) = f32x2{d1[2], d1[3]};
    if (g < 2) {
#pragma unroll
      for (int q = 0; q < 4; ++q) T[512 + 64 * g + j + 16 * q] = d2[q];
    } else if (lane == 32) {
      *(LDS f32x2*)(T + 640) = f32x2{d2[0], 0.f};
    }
  }
  lds_flag_set(F_GP + gw * 4);
  {
    float s0 = 0.f, s1 = 0.f;
#pragma unroll
    for (int j = 0; j < 4; ++j) {
      const float wl = bf_lo(gwv[j]), wh = bf_hi(gwv[j]);
      s0 = __builtin_fmaf(bf_lo(gx0[j]), wl, s0); s0 = __builtin_fmaf(bf_hi(gx0[j]), wh, s0);
      s1 = __builtin_fmaf(bf_lo(gx1[j]), wl, s1); s1 = __builtin_fmaf(bf_hi(gx1[j]), wh, s1);
    }
    auto sw = __builtin_amdgcn_permlane32_swap(f2u(s0), f2u(s1), false, false);
    float s = u2f(sw[0]) + u2f(sw[1]);
    auto sx = __builtin_amdgcn_permlane16_swap(f2u(s), f2u(s), false, false);
    s = u2f(sx[0]) + u2f(sx[1]);
    s = dpp_add<0x128>(s); s = dpp_add<0x124>(s); s = dpp_add<0x122>(s); s = dpp_add<0x121>(s);
    const float gv = round_bf16(__builtin_amdgcn_rcpf(1.0f + __expf(-round_bf16(s))));
    if ((lane & 31) == 0) st4_dev(ws, WS_G + (grow + (lane >> 5)) * 4, f2u(gv));
  }
  lds_wait<1, 0>(F_GP + (gw ^ 1) * 4);
#pragma unroll
  for (int it = 0; it < 2; ++it) {
    float k[9];
#pragma unroll
    for (int e = 0; e < 9; ++e) k[e] = lds_rf32(ea[it * 9 + e]) + lds_rf32(ea[it * 9 + e] + 8704);
#pragma unroll
    for (int e = 0; e < 4; ++e) { kv[2 * it][e] = pack_bf16(k[2 * e], k[2 * e + 1]); kv[2 * it + 1][e] = pack_bf16(k[2 * e + 1], k[2 * e + 2]); }
  }
  asm volatile("" : "+v"(kv[0]), "+v"(kv[1]), "+v"(kv[2]), "+v"(kv[3])::"memory");
  lds_flag_set(F_GE + gw * 4);
  STAMP(2);
  return get_tag();
}

DEV float rowmax16(f32x16 s) {
  float m = s[0];
#pragma unroll
  for (int i = 1; i < 16; ++i) m = __builtin_fmaxf(m, s[i]);
  return m;
}
DEV float halfmax(float m) {
  auto r = __builtin_amdgcn_permlane32_swap(f2u(m), f2u(m), false, false);
  return __builtin_fmaxf(u2f(r[0]), u2f(r[1]));
}
DEV f32x8 lo8(f32x16 v) { return __builtin_shufflevector(v, v, 0, 1, 2, 3, 4, 5, 6, 7); }
DEV f32x8 hi8(f32x16 v) { return __builtin_shufflevector(v, v, 8, 9, 10, 11, 12, 13, 14, 15); }
#define EXPS(S, a, b) _Pragma("unroll") for (int i_ = a; i_ < b; ++i_) { float e_ = S[i_]; asm volatile("v_exp_f32 %0, %0" : "+v"(e_)); S[i_] = e_; }
#define PV(V, P8) asm volatile("v_smfmac_f32_32x32x32_bf16 %0, %1, %2, %3" : "+v"(O) : "v"(V), "v"(P8), "v"(idx))
#define QK(D) asm volatile("v_mfma_f32_32x32x16_bf16 %0, %1, %2, %3" : "=v"(D) : "v"(kn), "v"(qf), "v"(negm))
#define TILE(C, P, T)                                              \
  EXPS(C, 0, 2); SB();                                             \
  PV(vh, hi8(P)); SB();                                            \
  vh = lds_r128(vb1 + (T) * KVS); SB();                            \
  EXPS(C, 2, 6); SB();                                             \
  QK(P); SB();                                                     \
  kn = lds_r128(kb + ((T) + 2) * KVS); SB();                       \
  EXPS(C, 6, 11); SB();                                            \
  PV(vl, lo8(C)); SB();                                            \
  vl = lds_r128(vb0 + ((T) + 1) * KVS); SB();                      \
  EXPS(C, 11, 16); SB();

DEV void b_attn(char* ws, int bid, int w, int lane PROF_ARG) {
  const int head = 4 * (bid & 7) + ((bid >> 3) & 3), qt = (bid >> 5) * 8 + w;
  const int r = lane & 31, h = lane >> 5;
  const u32x2 tag = get_tag();
  STAMP(3);
  if (w == 0) {
    lds_wait<4, 2>(F_GD);
    if (lane < 62) lds_w128(lane * KVS + 2048, u32x4{0x3f803f80u, 0x3f803f80u, 0x3f803f80u, 0x3f803f80u});
  }
  if (w == 4) {
    const int cq = (head >> 1) / 3, ck = (16 + (head >> 1)) / 3, cv = (32 + (head >> 1)) / 3;
    const int g = lane >> 4, i = lane & 15;
    poll1(ws, WS_FA + (g == 0 ? ck * 16 + i : g == 1 ? cv * 16 + i : g == 2 ? (bid >> 4) * 16 + i : cq * 16 + 2 * (bid >> 5) + (i & 1)) * 128, tag);
    lds_wait<4, 0>(F_GD);
    lds_flag_set(F_GO);
  } else {
    lds_wait<1, 1>(F_GO);
  }
  STAMP(4);
  PRIO(2);
  u32x4 qf;
  asm volatile("global_load_dwordx4 %0, %1, %2 sc1" : "=v"(qf) : "v"(lane * 16), "s"(ws + WS_QF + (head * 64 + qt) * 1024) : "memory");
  const char* kp = ws + WS_KF + (head * 64 + w) * 1024;
#pragma unroll
  for (int i = 0; i < 2; ++i) {
    dma16_dev((8 * i + w) * KVS, lane * 16, kp + i * 8192);
    dma16_dev((8 * i + w) * KVS + 1024, lane * 16, kp + (WS_VF - WS_KF) + i * 8192);
  }
  const unsigned ka = lane * 16;
  const unsigned va0 = r < 16 ? 1024 + h * 256 + r * 16 : 2048;
  const unsigned va1 = r < 16 ? 1536 + h * 256 + r * 16 : 2048;
  const int idx = (int)0xDDDDDDDDu;
  f32x16 zero16;
#pragma unroll
  for (int i = 0; i < 16; ++i) zero16[i] = 0.f;
  asm volatile("s_waitcnt vmcnt(2) lgkmcnt(0)" : "+v"(qf)::"memory");
  lds_flag_set(F_F0 + w * 4);
  lds_wait<8, 0>(F_F0);
  STAMP(5);
  float m = halfmax(rowmax16(mfma32(lds_r128(ka), qf, zero16)));
  f32x16 O;
  unsigned ev = 1u | (2u << 4) | (4u << 12) | (8u << 24);
  for (int pass = 0;; ++pass) {
    f32x16 negm;
#pragma unroll
    for (int i = 0; i < 16; ++i) negm[i] = -m;
    O = zero16;
    f32x16 S0 = mfma32(lds_r128(ka), qf, negm), S1 = zero16;
    u32x4 kn = lds_r128(ka + KVS);
    u32x4 vl = lds_r128(va0), vh = u32x4{0, 0, 0, 0};
    unsigned kb = ka, vb0 = va0, vb1 = va1;
    asm volatile("s_nop 11" : "+v"(S0), "+v"(S1), "+v"(O), "+v"(negm), "+v"(vh));
#pragma clang loop unroll(disable)
    for (int g = 0; g < 8; ++g) {
      asm volatile("" : "+s"(ev));
      TILE(S0, S1, 0)
      TILE(S1, S0, 1)
      if (ev & 1) { WAIT_VM(0); lds_flag_set(F_F1 + w * 4); }
      if (ev & 4) PRIO(1);
      if (ev & 8) PRIO(0);
      TILE(S0, S1, 2)
      TILE(S1, S0, 3)
      TILE(S0, S1, 4)
      TILE(S1, S0, 5)
      if (ev & 1) lds_wait<8, 0>(F_F1);
      if (ev & 2) lds_wait<4, 0>(F_FS);
      TILE(S0, S1, 6)
      TILE(S1, S0, 7)
      kb += 8 * KVS; vb0 += 8 * KVS; vb1 += 8 * KVS;
      ev >>= 4;
    }
    asm volatile("s_nop 1");
    PV(vh, hi8(S1));
    asm volatile("s_nop 15" : "+v"(O));
    if (pass == 1 || !__any(!(O[8] <= 1073741824.f))) break;
    float mm = -__builtin_inff();
    for (int t = 0; t < 64; ++t) mm = __builtin_fmaxf(mm, rowmax16(mfma32(lds_r128(ka + t * KVS), qf, zero16)));
    m = halfmax(mm);
  }
  STAMP(6);
  lds_flag_set(F_AD + w * 4);
  PRIO(3);
  const float inv = __builtin_amdgcn_rcpf(O[8]);
  u32x4 ov;
#pragma unroll
  for (int j = 0; j < 4; ++j) ov[j] = pack_bf16(O[2 * j] * inv, O[2 * j + 1] * inv);
  st16_dev(ws, WS_OF + (qt * 32 + head) * 1024 + lane * 16, ov);
  publish(ws, WS_FO + (qt * 32 + head) * 128, tag, lane);
  STAMP(7);
  const int T = (bid & 7) + 8 * ((bid >> 3) & 7);
  if (w == 7) {
    lds_wait<8, 0>(F_AD);
    poll1(ws, WS_FO + (T * 32 + (lane & 31)) * 128, tag);
    lds_flag_set(F_GO + 4);
  } else {
    if (w < 4) { PRIO(0); lds_wait<1, 8>(F_AD + 28); PRIO(3); }
    lds_wait<1, 0>(F_GO + 4);
  }
  STAMP(8);
#pragma unroll
  for (int i = 0; i < 4; ++i) dma16_dev(C_O + (4 * w + i) * 1024, lane * 16, ws + WS_OF + (T * 32 + 4 * w + i) * 1024);
  WAIT_VM(0);
  lds_flag_set(F_OC + w * 4);
  STAMP(9);
  if (bid == 0 && w == 0) {
    poll2(ws, WS_FY + lane * 128, WS_FY + (64 + lane) * 128, tag);
    poll2(ws, WS_FY + (128 + lane) * 128, WS_FY + (192 + lane) * 128, tag);
    if (lane == 0) st8_dev(ws, WS_EP, tag);
  }
}

typedef u32x4 __attribute__((aligned(4))) u32x4_u;
template <int C0, int C1, int C2, int C3>
DEV void conv_tiles(const char* kr, char* ws, unsigned yo, unsigned ub) {
  f32x4 a0 = {0, 0, 0, 0}, a1 = a0, a2 = a0, a3 = a0;
#pragma unroll
  for (int s = 0; s < 8 * (C3 + 1); ++s) {
    const u32x4 A = *(const u32x4_u*)(kr - 64 * s);
    if (s < 8 * (C0 + 1)) a0 = mfma16(A, lds_r128(ub + 512 * C0 + 4096 - 64 * s), a0);
    if (s < 8 * (C1 + 1)) a1 = mfma16(A, lds_r128(ub + 512 * C1 + 4096 - 64 * s), a1);
    if (s < 8 * (C2 + 1)) a2 = mfma16(A, lds_r128(ub + 512 * C2 + 4096 - 64 * s), a2);
    a3 = mfma16(A, lds_r128(ub + 512 * C3 + 4096 - 64 * s), a3);
  }
  st8_dev(ws, yo + 262144 * C0, u32x2{pack_bf16(a0[0], a0[1]), pack_bf16(a0[2], a0[3])});
  st8_dev(ws, yo + 262144 * C1, u32x2{pack_bf16(a1[0], a1[1]), pack_bf16(a1[2], a1[3])});
  st8_dev(ws, yo + 262144 * C2, u32x2{pack_bf16(a2[0], a2[1]), pack_bf16(a2[2], a2[3])});
  st8_dev(ws, yo + 262144 * C3, u32x2{pack_bf16(a3[0], a3[1]), pack_bf16(a3[2], a3[3])});
}

DEV void s_rest(const Params& P, u32x2 tag, int bid, int cw, int lane, const u32x4 (&kv)[4] PROF_ARG) {
  char* ws = P.ws;
  const int head = 4 * (bid & 7) + ((bid >> 3) & 3);
  const int d = 2 * bid + (cw >> 1);
  {
    lds_wait<4, 1>(F_GE);
    lds_wait<1, 1>(F_GO);
    STAMP(4);
    __builtin_amdgcn_s_sleep(4);
    const char* kp = ws + WS_KF + (head * 64 + 16 + cw) * 1024;
#pragma unroll
    for (int i = 0; i < 12; ++i) {
      dma16_dev((16 + 4 * i + cw) * KVS, lane * 16, kp + i * 4096);
      dma16_dev((16 + 4 * i + cw) * KVS + 1024, lane * 16, kp + (WS_VF - WS_KF) + i * 4096);
    }
    const unsigned ur = B_U + (cw >> 1) * 4608;
#pragma unroll
    for (int i = 0; i < 4; ++i) dma16_dev(ur + 512 + i * 1024, (lane >> 2) * 32768 + (lane & 3) * 16, ws + WS_XT + d * 64 + i * (16 * 32768));
    if (lane < 32) lds_w128(ur + lane * 16, u32x4{0, 0, 0, 0});
    if (cw >= 2 && lane == 0) lds_w128((60 + cw) * KVS + 2048, u32x4{0x3f803f80u, 0x3f803f80u, 0x3f803f80u, 0x3f803f80u});
    asm volatile("s_waitcnt vmcnt(4) lgkmcnt(0)" ::: "memory");
    lds_flag_set(F_FS + cw * 4);
    {
      char* kw = ws + WS_KR + (size_t)d * (KR_STRIDE * 4);
#pragma unroll
      for (int it = 0; it < 2; ++it) {
        *(u32x4*)(kw + (1024 * (cw & 1) + 512 * it + 8 * lane) * 2) = kv[2 * it];
        *(u32x4*)(kw + KR_STRIDE * 2 + (1024 * (cw & 1) + 512 * it + 8 * lane) * 2) = kv[2 * it + 1];
      }
      if ((cw & 1) && lane < 8) *(u32x4*)(kw + (lane >> 2) * (KR_STRIDE * 2) + 4096 + (lane & 3) * 16) = u32x4{0, 0, 0, 0};
    }
    WAIT_VM(0);
    lds_flag_set(F_KS + cw * 4);
    lds_wait<1, 0>(F_KS + (cw ^ 1) * 4);
    PRIO(3);
    STAMP(5);
    const int j = lane & 15, g = lane >> 4;
    const char* kr = ws + WS_KR + (size_t)d * (KR_STRIDE * 4) + ((j & 1) ? 0 : KR_STRIDE * 2) + ((2031 - j + 8 * g) & ~1) * 2;
    const unsigned ub = ur + 512 - 32 + 32 * j + 16 * g - 4096;
    const unsigned yo = WS_YT + ((j >> 1) * 512 + d) * 64 + (16 * (j & 1) + 4 * g) * 2;
    if (cw & 1) conv_tiles<2, 3, 4, 5>(kr, ws, yo, ub);
    else conv_tiles<0, 1, 6, 7>(kr, ws, yo, ub);
    lds_flag_set(F_CD + cw * 4);
    publish(ws, WS_FY + bid * 128 + cw * 8, tag, lane);
    STAMP(6);
  }
  const int T = (bid & 7) + 8 * ((bid >> 3) & 7), dt = 4 * (bid >> 6) + cw;
  const int g = lane >> 4, q4 = (lane & 15) >> 2, pp = lane & 3;
  const unsigned st = C_ST + cw * 7680;
  const unsigned wa = st + (4 * (g >> 1) + q4) * 64 + (g & 1) * 32 + pp * 8;
  unsigned vw[4];
#pragma unroll
  for (int i = 0; i < 4; ++i) vw[i] = (lane >> 2) * 1024 + (lane & 3) * 16 + i * (16384 - 1024);
  u32x4 wf[28];
  lds_wait<4, 1>(F_CD);
#pragma unroll
  for (int m = 0; m < 8; ++m) {
    dma16x4(st, vw, P.wo + dt * 64 + m * 65536);
    if (m == 7) break;
    WAIT_VM(0);
#pragma unroll
    for (int i = 0; i < 4; ++i) {
      const u32x2 lo = lds_tr(wa + i * 1024), hi = lds_tr(wa + i * 1024 + 512);
      wf[4 * m + i] = u32x4{lo[0], lo[1], hi[0], hi[1]};
    }
    asm volatile("s_waitcnt lgkmcnt(0)" : "+v"(wf[4 * m]), "+v"(wf[4 * m + 1]), "+v"(wf[4 * m + 2]), "+v"(wf[4 * m + 3])::"memory");
  }
  STAMP(7);
  const int tl = lane & 31, h = lane >> 5, t = 32 * T + tl;
  poll2(ws, WS_FY + (16 * dt + (lane >> 2)) * 128 + (lane & 3) * 8, WS_FY + (4 * T + ((lane >> 2) & 3)) * 128 + (lane & 3) * 8, tag);
  dma16_dev(st + 4096, lane * 16, ws + WS_YT + (T * 512 + 32 * dt) * 64);
  dma16_dev(st + 5120, lane * 16, ws + WS_YT + (T * 512 + 32 * dt) * 64 + 1024);
  float gt;
  asm volatile("global_load_dword %0, %1, %2 sc1\n\ts_waitcnt vmcnt(0)" : "=&v"(gt) : "v"(t * 4), "s"(ws + WS_G) : "memory");
  f32x16 acc;
#pragma unroll
  for (int i = 0; i < 16; ++i) acc[i] = 0.f;
  asm volatile("" : "+v"(acc));
  STAMP(8);
  const unsigned oa = C_O + lane * 16;
#pragma unroll
  for (int m = 0; m < 8; ++m) {
    if (m == 0) { PRIO(0); lds_wait<1, 8>(F_AD + 28); PRIO(3); lds_wait<8, 0>(F_OC); STAMP(9); }
    u32x4 of[4];
#pragma unroll
    for (int i = 0; i < 4; ++i) of[i] = lds_r128(oa + (4 * m + i) * 1024);
    if (m < 7) {
#pragma unroll
      for (int i = 0; i < 4; ++i) MFMA32(acc, wf[4 * m + i], of[i]);
    } else {
#pragma unroll
      for (int i = 0; i < 4; ++i) {
        const u32x2 lo = lds_tr(wa + i * 1024), hi = lds_tr(wa + i * 1024 + 512);
        const u32x4 wl = {lo[0], lo[1], hi[0], hi[1]};
        MFMA32(acc, wl, of[i]);
      }
    }
  }
  u32x2 y[4];
#pragma unroll
  for (int m = 0; m < 4; ++m) y[m] = lds_tr(st + 4096 + (8 * m + 4 * (g >> 1) + q4) * 64 + (g & 1) * 32 + pp * 8);
  asm volatile("s_nop 15" : "+v"(acc));
  STAMP(10);
  const float omg = 1.0f - gt;
#pragma unroll
  for (int m = 0; m < 4; m += 2) {
    u32x2 a, b;
#pragma unroll
    for (int e = 0; e < 2; ++e) {
      a[e] = pack_bf16(__builtin_fmaf(gt, acc[4 * m + 2 * e], omg * bf_lo(y[m][e])), __builtin_fmaf(gt, acc[4 * m + 2 * e + 1], omg * bf_hi(y[m][e])));
      b[e] = pack_bf16(__builtin_fmaf(gt, acc[4 * m + 4 + 2 * e], omg * bf_lo(y[m + 1][e])), __builtin_fmaf(gt, acc[4 * m + 5 + 2 * e], omg * bf_hi(y[m + 1][e])));
    }
    auto s0 = __builtin_amdgcn_permlane32_swap(a[0], b[0], false, false);
    auto s1 = __builtin_amdgcn_permlane32_swap(a[1], b[1], false, false);
    const unsigned r0 = s0[0], r1 = s1[0], r2 = s0[1], r3 = s1[1];
    *(u32x4*)(P.out + (size_t)(t * 1024 + (32 * dt + 8 * m + 8 * h) * 2)) = u32x4{r0, r1, r2, r3};
  }
}

__global__ __launch_bounds__(768, 3) void gated_mix_kernel(Params P) {
  const int bid = blockIdx.x, lane = threadIdx.x & 63;
  const int w = __builtin_amdgcn_readfirstlane(threadIdx.x >> 6);
  PROF_DECL;
  if (w < 8) {
    const int xcd = bid & 7, j = bid >> 3;
    const int tb = 4 * (xcd >> 1) + (j & 3), c = 8 * (xcd & 1) + (j >> 2);
    const int s0 = 0;
    if (w < 4) {
      a_load(P.x, P.wqkv, tb, c, s0, w, lane PROF_PASS);
    } else {
      PRIO(1);
      if (c < 10) a_gemm<0>(P.ws, tb, c, s0, w - 4, lane PROF_PASS);
      else if (c == 10) a_gemm<1>(P.ws, tb, c, s0, w - 4, lane PROF_PASS);
      else a_gemm<3>(P.ws, tb, c, s0, w - 4, lane PROF_PASS);
    }
    b_attn(P.ws, bid, w, lane PROF_PASS);
  } else {
    PRIO(3);
    u32x4 kv[4];
    const u32x2 tag = s_gen(P, bid, w - 8, lane, kv PROF_PASS);
    s_rest(P, tag, bid, w - 8, lane, kv PROF_PASS);
  }
  PROF_END(0, P.ws);
}

extern "C" void kernel_launch(void* const* d_in, const int* in_sizes, int n_in, void* d_out, int out_size, void* d_ws,
                              size_t ws_size, hipStream_t stream) {
  (void)in_sizes; (void)n_in; (void)out_size; (void)ws_size;
  (void)hipFuncSetAttribute((const void*)gated_mix_kernel, hipFuncAttributeMaxDynamicSharedMemorySize, LDS_BYTES);
  Params P;
  P.x = (const char*)d_in[0]; P.wqkv = (const char*)d_in[1]; P.wo = (const char*)d_in[2];
  P.Are = (const char*)d_in[4]; P.Aim = (const char*)d_in[5]; P.Bre = (const char*)d_in[6]; P.Bim = (const char*)d_in[7];
  P.Cre = (const char*)d_in[8]; P.Cim = (const char*)d_in[9]; P.logdt = (const char*)d_in[10]; P.dskip = (const char*)d_in[11];
  P.gatew = (const char*)d_in[12]; P.ws = (char*)d_ws; P.out = (char*)d_out;
  gated_mix_kernel<<<256, 768, LDS_BYTES, stream>>>(P);
}
```

```cpp
#include <hip/hip_runtime.h>
#include <hip/hip_bf16.h>
#include <stdint.h>

typedef unsigned short u16;
typedef unsigned long long u64;
typedef __attribute__((ext_vector_type(16))) float f32x16;
typedef __attribute__((ext_vector_type(8))) float f32x8;
typedef __attribute__((ext_vector_type(4))) float f32x4;
typedef __attribute__((ext_vector_type(2))) float f32x2;
typedef __attribute__((ext_vector_type(2))) __bf16 bf16x2;
typedef __attribute__((ext_vector_type(8))) __bf16 bf16x8;
typedef __attribute__((ext_vector_type(16))) __bf16 bf16x16;
typedef __attribute__((ext_vector_type(4))) short s16x4;
typedef __attribute__((ext_vector_type(4))) unsigned u32x4;
typedef __attribute__((ext_vector_type(2))) unsigned u32x2;
#define LDS __attribute__((address_space(3)))
#define DEV __device__ __forceinline__

constexpr unsigned WS_QF = 0;
constexpr unsigned WS_KF = 2u << 20;
constexpr unsigned WS_VF = 4u << 20;
constexpr unsigned WS_XT = 6u << 20;
constexpr unsigned WS_G  = 8u << 20;
constexpr unsigned WS_EP = WS_G + 8192;
constexpr unsigned WS_FA = WS_G + 16384;
constexpr unsigned WS_FX = WS_FA + 32768;
constexpr unsigned WS_FY = WS_FX + 32768;
constexpr unsigned WS_FO = WS_FY + 32768;
#define NKERN 1
#define F3_LAYOUT

constexpr unsigned A_W = 0;
constexpr unsigned A_X = 61440;
constexpr unsigned B_U = 143360;
constexpr unsigned B_TAB = 152576;
DEV unsigned conv_region(int p) { return p ? 108160u : 33280u; }
constexpr unsigned KVS = 2080;
constexpr int NKA = 36;
constexpr unsigned B_EX = 0;
constexpr unsigned C_O = 20480;
constexpr unsigned C_ST = 133376;
constexpr unsigned FL = 163328;
constexpr unsigned F_LANDED = FL, F_EX = FL + 32, F_DONE = FL + 64, F_ONES = FL + 112, F_XD = FL + 128, F_XGO = FL + 132, F_GO = FL + 136,
                   F_TAGOK = FL + 140, F_GP = FL + 144, F_GD = FL + 176, F_F0 = FL + 192, F_F1 = FL + 224,
                   F_FS = FL + 256, F_CD = FL + 272, F_AD = FL + 288, F_OC = FL + 320, F_TAG = FL + 352, F_PD = FL + 368, F_T1 = FL + 384;
constexpr unsigned LDS_BYTES = 163840;

#define PROF_DECL
#define STAMP(i)
#define STAMPN(i)
#define RSTAMP(i)
#define PROF_ARG
#define PROF_PASS
#define PROF_END(kid, ws_)

DEV unsigned pack_bf16(float lo, float hi) {
  f32x2 a = {lo, hi};
  return __builtin_bit_cast(unsigned, __builtin_convertvector(a, bf16x2));
}
DEV float u2f(unsigned u) { return __builtin_bit_cast(float, u); }
DEV unsigned f2u(float f) { return __builtin_bit_cast(unsigned, f); }
DEV float bf_lo(unsigned u) { return u2f(u << 16); }
DEV float bf_hi(unsigned u) { return u2f(u & 0xffff0000u); }
DEV float round_bf16(float f) { return bf_lo(pack_bf16(f, f)); }

#define WAIT_VM(n) asm volatile("s_waitcnt vmcnt(" #n ")" ::: "memory")
#define WAIT_LGKM0() asm volatile("s_waitcnt lgkmcnt(0)" ::: "memory")
#define CBAR() asm volatile("" ::: "memory")
#define SB() __builtin_amdgcn_sched_barrier(0)
#define PRIO(n) __builtin_amdgcn_s_setprio(n)
DEV void dma16(unsigned lds_addr, unsigned voff, const void* sbase) {
  asm volatile("s_mov_b32 m0, %0\n\ts_nop 0\n\tglobal_load_lds_dwordx4 %1, %2" ::"s"(lds_addr), "v"(voff), "s"(sbase) : "memory");
}
DEV void dma16_dev(unsigned lds_addr, unsigned voff, const void* sbase) {
  asm volatile("s_mov_b32 m0, %0\n\ts_nop 0\n\tglobal_load_lds_dwordx4 %1, %2 sc1" ::"s"(lds_addr), "v"(voff), "s"(sbase) : "memory");
}
DEV void dma16x4(unsigned lds_addr, const unsigned* v, const void* sbase) {
  asm volatile("s_mov_b32 m0, %0\n\ts_nop 0\n\tglobal_load_lds_dwordx4 %1, %5\n\tglobal_load_lds_dwordx4 %2, %5 offset:1024\n\t"
               "global_load_lds_dwordx4 %3, %5 offset:2048\n\tglobal_load_lds_dwordx4 %4, %5 offset:3072"
               ::"s"(lds_addr), "v"(v[0]), "v"(v[1]), "v"(v[2]), "v"(v[3]), "s"(sbase) : "memory");
}
DEV u32x4 lds_r128(unsigned a) { return *(LDS u32x4*)(size_t)a; }
DEV void lds_w128(unsigned a, u32x4 v) { *(LDS u32x4*)(size_t)a = v; }
DEV void lds_w32(unsigned a, unsigned v) { *(LDS unsigned*)(size_t)a = v; }
DEV float lds_rf32(unsigned a) { return *(LDS float*)(size_t)a; }
DEV u32x2 lds_tr(unsigned a) { return __builtin_bit_cast(u32x2, __builtin_amdgcn_ds_read_tr16_b64_v4i16((LDS s16x4*)(size_t)a)); }
DEV bf16x8 as_bf8(u32x4 v) { return __builtin_bit_cast(bf16x8, v); }
DEV f32x16 mfma32(u32x4 a, u32x4 b, f32x16 c) { return __builtin_amdgcn_mfma_f32_32x32x16_bf16(as_bf8(a), as_bf8(b), c, 0, 0, 0); }
DEV f32x4 mfma16(u32x4 a, u32x4 b, f32x4 c) { return __builtin_amdgcn_mfma_f32_16x16x32_bf16(as_bf8(a), as_bf8(b), c, 0, 0, 0); }
#define MFMA32(D, A, B) asm volatile("v_mfma_f32_32x32x16_bf16 %0, %1, %2, %0" : "+v"(D) : "v"(A), "v"(B))
DEV void st16_dev(const void* base, unsigned off, u32x4 v) { asm volatile("global_store_dwordx4 %0, %1, %2 sc1\n\ts_nop 1" ::"v"(off), "v"(v), "s"(base) : "memory"); }
DEV void st8_dev(const void* base, unsigned off, u32x2 v) { asm volatile("global_store_dwordx2 %0, %1, %2 sc1" ::"v"(off), "v"(v), "s"(base) : "memory"); }
DEV void st4_dev(const void* base, unsigned off, unsigned v) { asm volatile("global_store_dword %0, %1, %2 sc1" ::"v"(off), "v"(v), "s"(base) : "memory"); }

DEV void lds_flag_set(unsigned a) { asm volatile("ds_write_b32 %0, %1" ::"v"(a), "v"(1u) : "memory"); }
template <int NW, int SLEEP> DEV void lds_wait(unsigned a) {
#pragma clang loop unroll(disable)
  for (int n = 0; n < (1 << 21); ++n) {
    unsigned ok;
    if (NW == 8) {
      u32x4 v, u;
      asm volatile("ds_read_b128 %0, %2\n\tds_read_b128 %1, %2 offset:16\n\ts_waitcnt lgkmcnt(0)" : "=&v"(v), "=&v"(u) : "v"(a) : "memory");
      ok = v[0] & v[1] & v[2] & v[3] & u[0] & u[1] & u[2] & u[3];
    } else if (NW == 4) {
      u32x4 v;
      asm volatile("ds_read_b128 %0, %1\n\ts_waitcnt lgkmcnt(0)" : "=&v"(v) : "v"(a) : "memory");
      ok = v[0] & v[1] & v[2] & v[3];
    } else {
      asm volatile("ds_read_b32 %0, %1\n\ts_waitcnt lgkmcnt(0)" : "=&v"(ok) : "v"(a) : "memory");
    }
    if (__builtin_amdgcn_readfirstlane(ok)) break;
    if (SLEEP) __builtin_amdgcn_s_sleep(SLEEP);
  }
}
#define SL1 0
DEV u32x4 lds_peek4(unsigned a) { return *(volatile LDS u32x4*)(size_t)a; }
DEV u32x2 get_tag() {
  lds_wait<1, 1>(F_TAGOK);
  return *(volatile LDS u32x2*)(size_t)F_TAG;
}
#define PSLEEP 8
DEV void publish(char* ws, unsigned off, u32x2 tag, int lane) {
  WAIT_VM(0);
  if (lane == 0) st8_dev(ws, off, tag);
}
DEV void poll1(const char* ws, unsigned off, u32x2 tag) {
#pragma clang loop unroll(disable)
  for (int n = 0; n < (1 << 17); ++n) {
    u32x2 a;
    asm volatile("global_load_dwordx2 %0, %1, %2 sc1\n\ts_waitcnt vmcnt(0)" : "=&v"(a) : "v"(off), "s"(ws) : "memory");
    if (!__any(a[0] != tag[0] || a[1] != tag[1])) break;
    __builtin_amdgcn_s_sleep(PSLEEP);
  }
}
DEV void poll2(const char* ws, unsigned off0, unsigned off1, u32x2 tag) {
#pragma clang loop unroll(disable)
  for (int n = 0; n < (1 << 17); ++n) {
    u32x2 a, b;
    asm volatile("global_load_dwordx2 %0, %2, %4 sc1\n\tglobal_load_dwordx2 %1, %3, %4 sc1\n\ts_waitcnt vmcnt(0)"
                 : "=&v"(a), "=&v"(b) : "v"(off0), "v"(off1), "s"(ws) : "memory");
    if (!__any(a[0] != tag[0] || a[1] != tag[1] || b[0] != tag[0] || b[1] != tag[1])) break;
    __builtin_amdgcn_s_sleep(PSLEEP);
  }
}

#define STAG 6
DEV void a_load(const char* x, const char* wq, char* ws, int bid, int tb, int c, int s0, int l, int lane PROF_ARG) {
  PRIO(2);
  unsigned vw[12], vx[16];
#pragma unroll
  for (int n = 0; n < 12; ++n) {
    const int m = 64 * (n % 3) + lane, row = m / 12;
    vw[n] = row * 3072 + (m - 12 * row) * 16 + (n / 3) * 49152 + 3072 - (n & 3) * 1024;
  }
#pragma unroll
  for (int n = 0; n < 16; ++n)
    vx[n] = (lane >> 3) * 1024 + (((lane & 7) ^ (4 * (n & 1) + (lane >> 4))) << 4) + (n >> 2) * 32768 + (n & 3) * 7168;
  wq += 192 * c - 3072;
  x += (size_t)(128 * tb) * 1024;
  for (int n = 0; n < l; ++n) __builtin_amdgcn_s_sleep(STAG);
  auto issue = [&](int i) {
    const int s = (s0 + i) & 7, sl = i % 5;
#pragma unroll
    for (int q = 0; q < 4; ++q) {
      dma16x4(A_X + q * 20480 + sl * 4096, vx + 4 * q, x + s * 128);
      if (q < 3) dma16x4(A_W + sl * 12288 + q * 4096, vw + 4 * q, wq + (size_t)s * (64 * 3072));
    }
  };
  STAMP(16);
  issue(l);
  STAMP(17);
  WAIT_VM(0);
  lds_flag_set(F_LANDED + l * 4);
  STAMP(18);
  if (l == 0) issue(4);
  const int ix = (c & 7) >> 1, xb = 2 * ((s0 + ix) & 7) + (c & 1);
  if (l == ix) {
    const int g = lane >> 4, q4 = (lane & 15) >> 2, pp = lane & 3;
    u32x2 t[4][4];
#pragma unroll
    for (int m = 0; m < 4; ++m) {
      const int row = 8 * g + 4 * (m & 1) + q4;
      const int ch = (4 * (c & 1) + 2 * (m >> 1) + (pp >> 1)) ^ ((row >> 1) & 7);
#pragma unroll
      for (int q = 0; q < 4; ++q) t[q][m] = lds_tr(A_X + l * 4096 + row * 128 + ch * 16 + (pp & 1) * 8 + q * 20480);
    }
    lds_flag_set(F_XD);
    const unsigned off = WS_XT + (4 * tb * 512 + 32 * xb + (lane & 15)) * 64 + g * 16;
#pragma unroll
    for (int q = 0; q < 4; ++q) {
      st16_dev(ws, off + q * 32768, u32x4{t[q][0][0], t[q][0][1], t[q][1][0], t[q][1][1]});
      st16_dev(ws, off + q * 32768 + 1024, u32x4{t[q][2][0], t[q][2][1], t[q][3][0], t[q][3][1]});
    }
  }
  if (l > 0) {
    lds_wait<4, SL1>(F_DONE + (l - 1) * 16);
    if (l - 1 == ix) lds_wait<1, 0>(F_XD);
    STAMP(19);
    issue(l + 4);
  }
  STAMP(20);
  WAIT_VM(0);
  lds_flag_set(F_LANDED + (l + 4) * 4);
  STAMP(21);
  if (l == ix) {
    const u32x2 tag = get_tag();
    if (lane == 0) st8_dev(ws, WS_FX + (xb * 16 + tb) * 128, tag);
  }
  PRIO(0);
  if (l == 1) {
    const u32x2 tag = get_tag();
    lds_w128(B_U + (lane >> 5) * 4608 + (lane & 31) * 16, u32x4{0, 0, 0, 0});
    poll1(ws, WS_FX + ((bid >> 4) * 16 + (lane & 15)) * 128, tag);
#pragma unroll
    for (int i = 0; i < 8; ++i)
      dma16_dev(B_U + (i >> 2) * 4608 + 512 + (i & 3) * 1024, (lane >> 2) * 32768 + (lane & 3) * 16,
                ws + WS_XT + (2 * bid + (i >> 2)) * 64 + (i & 3) * (16 * 32768));
    WAIT_VM(0);
    lds_flag_set(F_XGO);
    STAMP(22);
  }
}

template <int NV>
DEV void a_gemm(char* ws, const char* x, const char* wq, int tb, int c, int s0, int w, int lane PROF_ARG) {
  const int r = lane & 31, h = lane >> 5;
  const unsigned xr = A_X + w * 20480;
  unsigned xa[4];
#pragma unroll
  for (int ks = 0; ks < 4; ++ks) { xa[ks] = xr + r * 128 + ((((h ^ (r >> 1)) & 7) << 4) ^ (ks << 5)); asm volatile("" : "+v"(xa[ks])); }
  const int g = lane >> 4, q4 = (lane & 15) >> 2, pp = lane & 3;
  unsigned wa = A_W + (8 * (g >> 1) + q4) * 192 + (g & 1) * 32 + pp * 8;
  asm volatile("" : "+v"(wa));
  const int T = 4 * tb + w;
  f32x16 acc[3];
#pragma unroll
  for (int p = 0; p < 3; ++p)
#pragma unroll
    for (int i = 0; i < 16; ++i) acc[p][i] = 0.f;
  asm volatile("" : "+v"(acc[0]), "+v"(acc[1]), "+v"(acc[2]));
  unsigned pf0 = 0, pf1 = 0;
  const u32x2 tag = get_tag();
  asm volatile("s_waitcnt vmcnt(0)" : "+v"(pf0), "+v"(pf1)::"memory");
  lds_wait<1, SL1>(F_LANDED);
  STAMP(1); RSTAMP(24);
  constexpr int PF = 2, NB = PF + 1;
  u32x4 xf[NB];
  u32x2 wl[NB][3], wh[NB][3];
  auto rdx = [&](int k) { xf[k % NB] = lds_r128(xa[k & 3] + ((k >> 2) % 5) * 4096); };
  auto rdw = [&](int k, int p) {
    wl[k % NB][p] = lds_tr(wa + ((k >> 2) % 5) * 12288 + (k & 3) * 3072 + p * 64);
    wh[k % NB][p] = lds_tr(wa + ((k >> 2) % 5) * 12288 + (k & 3) * 3072 + p * 64 + 768);
  };
#pragma unroll
  for (int k = 0; k < PF; ++k) { rdx(k); rdw(k, 0); rdw(k, 1); rdw(k, 2); }
  unsigned pk = 0;
#pragma unroll
  for (int k = 0; k < 32; ++k) {
    const int i = k >> 2, ks = k & 3;
    if (ks == 0) STAMPN(16 + i);
    SB();
#pragma unroll
    for (int p = 0; p < 3; ++p) {
      const u32x4 wf = {wl[k % NB][p][0], wl[k % NB][p][1], wh[k % NB][p][0], wh[k % NB][p][1]};
      if (p >= 3 - NV) MFMA32(acc[p], xf[k % NB], wf); else MFMA32(acc[p], wf, xf[k % NB]);
      SB();
      if (ks == 4 - PF && p == 0 && i < 7) {
        if (__builtin_expect(!__builtin_amdgcn_readfirstlane(pk), 0)) lds_wait<1, SL1>(F_LANDED + (i + 1) * 4);
        CBAR();
      }
      if (k + PF < 32) { if (p == 0) rdx(k + PF); rdw(k + PF, p); }
      if (ks == 3 - PF && p == 1 && i < 7) pk = *(volatile LDS unsigned*)(size_t)(F_LANDED + (i + 1) * 4);
      if (ks == 3 - PF && p == 2) {
        if (i < 3) lds_flag_set(F_DONE + i * 16 + w * 4);
        else if (i == 7) lds_flag_set(F_GD + w * 4);
      }
      SB();
    }
  }
  asm volatile("s_nop 15" : "+v"(acc[0]), "+v"(acc[1]), "+v"(acc[2]));
  STAMP(2); RSTAMP(25);
#pragma unroll
  for (int p = 0; p < 3; ++p) {
    const int G = 3 * c + p, hp = G & 15;
    if (p >= 3 - NV) {
      const unsigned off = WS_VF + ((2 * hp + (r >> 4)) * 64 + T) * 1024 + h * 256 + (r & 15) * 16;
#pragma unroll
      for (int s = 0; s < 2; ++s) {
        u32x4 v;
#pragma unroll
        for (int j = 0; j < 4; ++j) v[j] = pack_bf16(acc[p][8 * s + 2 * j], acc[p][8 * s + 2 * j + 1]);
        st16_dev(ws, off + s * 512, v);
      }
    } else {
      const bool isq = G < 16;
      const float sc = isq ? 0.36067376022224085f : 1.0f;
      const int slotr = isq ? r : ((r & 19) | ((r & 4) << 1) | ((r & 8) >> 1));
      const unsigned off = (isq ? WS_QF : WS_KF) + (2 * hp * 64 + T) * 1024 + h * 512 + slotr * 16;
#pragma unroll
      for (int s = 0; s < 2; ++s) {
        u32x4 v;
#pragma unroll
        for (int j = 0; j < 4; ++j) v[j] = pack_bf16(acc[p][8 * s + 2 * j] * sc, acc[p][8 * s + 2 * j + 1] * sc);
        st16_dev(ws, off + s * 65536, v);
      }
    }
  }
  WAIT_VM(0);
  lds_flag_set(F_PD + w * 4);
  if (w == 0) {
    lds_wait<4, 0>(F_PD);
    if (lane == 0) st8_dev(ws, WS_FA + (c * 16 + tb) * 128, tag);
  }
}

struct cf { float r, i; };
DEV cf cmul(cf a, cf b) {
  cf o;
  asm("v_mul_f32 %0, %3, %5\n\tv_mul_f32 %1, %3, %4\n\tv_fma_f32 %0, %2, %4, -%0\n\tv_fmac_f32 %1, %2, %5"
      : "=&v"(o.r), "=&v"(o.i) : "v"(a.r), "v"(a.i), "v"(b.r), "v"(b.i));
  return o;
}
DEV void cmul2(cf& r0, cf a0, cf b0, cf& r1, cf a1, cf b1) {
  cf o, q;
  asm("v_mul_f32 %0, %5, %7\n\tv_mul_f32 %1, %5, %6\n\tv_mul_f32 %2, %9, %11\n\tv_mul_f32 %3, %9, %10\n\t"
      "v_fma_f32 %0, %4, %6, -%0\n\tv_fmac_f32 %1, %4, %7\n\tv_fma_f32 %2, %8, %10, -%2\n\tv_fmac_f32 %3, %8, %11"
      : "=&v"(o.r), "=&v"(o.i), "=&v"(q.r), "=&v"(q.i)
      : "v"(a0.r), "v"(a0.i), "v"(b0.r), "v"(b0.i), "v"(a1.r), "v"(a1.i), "v"(b1.r), "v"(b1.i));
  r0 = o; r1 = q;
}
DEV float fadd1(float a, float b) { float o; asm("v_add_f32 %0, %1, %2" : "=v"(o) : "v"(a), "v"(b)); return o; }
DEV cf cseed(float a2, float br, float L) {
  const float e = __builtin_amdgcn_exp2f(a2 * L);
  const float ph = __builtin_amdgcn_fractf(br * L);
  return cf{e * __builtin_amdgcn_cosf(ph), e * __builtin_amdgcn_sinf(ph)};
}
template <int CTRL> DEV float dpp_add(float v) {
  return v + u2f(__builtin_amdgcn_update_dpp(0, f2u(v), CTRL, 0xf, 0xf, true));
}
struct Params {
  const char *x, *wqkv, *wo, *Are, *Aim, *Bre, *Bim, *Cre, *Cim, *logdt, *dskip, *gatew;
  char *ws, *out;
};

DEV u32x2 s_gen(const Params& P, int bid, int gw, int lane, u32x4 (&kv)[4] PROF_ARG) {
  char* ws = P.ws;
  const int d = 2 * bid + (gw >> 1), hw = gw & 1;
  const int pi = d * 128 + 64 * hw + lane;
  unsigned rdt, rds, rAre, rAim, rBre, rBim, rCre, rCim;
  u32x2 tag;
#define LDU16(dst, base, off) asm volatile("global_load_ushort %0, %1, %2" : "=v"(dst) : "v"(off), "s"(base) : "memory")
#define LD16(dst, base, off) asm volatile("global_load_dwordx4 %0, %1, %2" : "=v"(dst) : "v"(off), "s"(base) : "memory")
  asm volatile("global_load_dwordx2 %0, %1, %2 sc1" : "=v"(tag) : "v"(WS_EP), "s"(ws) : "memory");
  LDU16(rdt, P.logdt, d * 2); LDU16(rAre, P.Are, pi * 2); LDU16(rAim, P.Aim, pi * 2); LDU16(rBre, P.Bre, pi * 2);
  LDU16(rBim, P.Bim, pi * 2); LDU16(rCre, P.Cre, pi * 2); LDU16(rCim, P.Cim, pi * 2); LDU16(rds, P.dskip, d * 2);
  const int grow = 8 * bid + 2 * gw;
  u32x4 gx0, gx1, gwv;
  LD16(gx0, P.x, grow * 1024 + lane * 16); LD16(gx1, P.x, grow * 1024 + 1024 + lane * 16); LD16(gwv, P.gatew, lane * 16);
  PRIO(0);
  const unsigned reg = conv_region(gw >> 1) + hw * 17408, tab = B_TAB + (gw >> 1) * 5128;
  unsigned ea[16];
#pragma unroll
  for (int it = 0; it < 2; ++it)
#pragma unroll
    for (int e = 0; e < 8; e += 2) {
      const int l0 = 2047 - (1024 * hw + 512 * it + 8 * lane) - e;
      const unsigned pr = pack_bf16((float)l0, (float)(l0 - 1));
      ea[it * 8 + e] = tab + (l0 < 128 ? l0 : (int)(pr & 0xffffu) - 0x4280) * 8;
      ea[it * 8 + e + 1] = tab + (l0 - 1 < 128 ? l0 - 1 : (int)(pr >> 16) - 0x4280) * 8;
    }
#pragma unroll
  for (int i = 0; i < 16; ++i) asm volatile("" : "+v"(ea[i]));
  asm volatile("s_waitcnt vmcnt(3)" : "+v"(tag), "+v"(rdt), "+v"(rds), "+v"(rAre), "+v"(rAim), "+v"(rBre), "+v"(rBim), "+v"(rCre),
               "+v"(rCim)::"memory");
  if (gw == 0) {
    tag[0] += 1;
    *(volatile LDS u32x2*)(size_t)F_TAG = tag;
    lds_flag_set(F_TAGOK);
  }
  STAMP(1);
  const float dt = round_bf16(expf(bf_lo(rdt)));
  const float a2 = (dt * bf_lo(rAre)) * 1.4426950408889634f;
  const float br = (dt * bf_lo(rAim)) * 0.15915494309189535f;
  const cf w1 = cseed(a2, br, 1.f), W = cseed(a2, br, 128.f);
  cf w8c = cseed(a2, br, 8.f); w8c.i = -w8c.i;
  cf R = cmul(cf{bf_lo(rCre), -bf_lo(rCim)}, cf{dt * bf_lo(rBre), -(dt * bf_lo(rBim))});
  unsigned rv[16], lv[41];
#define PK(v) pack_bf16((v).r, (v).i)
  cf p[8], Wp[17];
  p[0] = cf{1.f, 0.f}; p[1] = w1; Wp[1] = W;
  lv[0] = 0x3f80u; lv[1] = PK(w1); lv[8] = PK(W); rv[0] = PK(R);
#pragma unroll
  for (int i = 2; i < 8; ++i) { cmul2(p[i], p[i - 1], w1, R, R, w8c); lv[i] = PK(p[i]); rv[i - 1] = PK(R); }
#pragma unroll
  for (int i = 1; i < 8; ++i) { cf t; cmul2(t, W, p[i], R, R, w8c); lv[8 + i] = PK(t); rv[6 + i] = PK(R); }
  cmul2(Wp[2], W, W, R, R, w8c); rv[14] = PK(R);
  cmul2(Wp[3], Wp[2], W, R, R, w8c); rv[15] = PK(R);
  lv[16] = PK(Wp[2]); lv[20] = PK(Wp[3]);
#pragma unroll
  for (int i = 1; i < 4; ++i) { cf t, u; cmul2(t, Wp[2], p[2 * i], u, Wp[3], p[2 * i]); lv[16 + i] = PK(t); lv[20 + i] = PK(u); }
  cmul2(Wp[4], Wp[2], Wp[2], Wp[5], Wp[3], Wp[2]);
  cmul2(Wp[6], Wp[3], Wp[3], Wp[7], Wp[4], Wp[3]);
#pragma unroll
  for (int a = 0; a < 4; a += 2) {
    cf t, u; cmul2(t, Wp[4 + a], p[4], u, Wp[5 + a], p[4]);
    lv[24 + 2 * a] = PK(Wp[4 + a]); lv[25 + 2 * a] = PK(t); lv[26 + 2 * a] = PK(Wp[5 + a]); lv[27 + 2 * a] = PK(u);
  }
#pragma unroll
  for (int a = 8; a < 16; a += 2) { cmul2(Wp[a], Wp[a - 4], Wp[4], Wp[a + 1], Wp[a - 3], Wp[4]); lv[24 + a] = PK(Wp[a]); lv[25 + a] = PK(Wp[a + 1]); }
  Wp[16] = cmul(Wp[8], Wp[8]); lv[40] = PK(Wp[16]);
#undef PK
#pragma unroll
  for (int i = 0; i < 16; ++i) asm volatile("" : "+v"(rv[i]));
#pragma unroll
  for (int i = 1; i < 41; ++i) asm volatile("" : "+v"(lv[i]));
  asm volatile("s_waitcnt vmcnt(0)" : "+v"(gx0), "+v"(gx1), "+v"(gwv)::"memory");
  {
    float s0 = 0.f, s1 = 0.f;
#pragma unroll
    for (int j = 0; j < 4; ++j) {
      const float wl = bf_lo(gwv[j]), wh = bf_hi(gwv[j]);
      s0 = __builtin_fmaf(bf_lo(gx0[j]), wl, s0); s0 = __builtin_fmaf(bf_hi(gx0[j]), wh, s0);
      s1 = __builtin_fmaf(bf_lo(gx1[j]), wl, s1); s1 = __builtin_fmaf(bf_hi(gx1[j]), wh, s1);
    }
    auto sw = __builtin_amdgcn_permlane32_swap(f2u(s0), f2u(s1), false, false);
    float s = u2f(sw[0]) + u2f(sw[1]);
    auto sx = __builtin_amdgcn_permlane16_swap(f2u(s), f2u(s), false, false);
    s = u2f(sx[0]) + u2f(sx[1]);
    s = dpp_add<0x128>(s); s = dpp_add<0x124>(s); s = dpp_add<0x122>(s); s = dpp_add<0x121>(s);
    const float gv = round_bf16(__builtin_amdgcn_rcpf(1.0f + __expf(-round_bf16(s))));
    if ((lane & 31) == 0) st4_dev(ws, WS_G + (grow + (lane >> 5)) * 4, f2u(gv));
  }
  STAMP(16);
  const int j = lane & 15, g = lane >> 4;
  unsigned t0a = tab + hw * 4 + (128 * (g >> 1) + 8 * j + 4 * (g & 1)) * 8;
  unsigned t1a = tab + hw * 4 + (g < 2 ? 256 + 64 * g + 4 * j : 384 + 64 * (g - 2) + 2 * j) * 8, t1b = t1a + (g < 2 ? 16 : 256);
  unsigned t2a = tab + hw * 4 + (512 + 64 * g + j) * 8;
  const float ds0 = (lane == 0 && hw == 0) ? bf_lo(rds) : 0.f;
  asm volatile("" : "+v"(t0a), "+v"(t1a), "+v"(t1b), "+v"(t2a));
  tag = get_tag();
  lds_wait<4, 1>(F_GD);
  PRIO(1);
  STAMP(3);
  const unsigned wa = reg + lane * 4, ra = reg + (lane & 15) * 272 + (lane >> 4) * 16;
#pragma unroll
  for (int i = 0; i < 16; ++i) lds_w32(wa + i * 272, rv[i]);
#pragma unroll
  for (int i = 0; i < 41; ++i) lds_w32(wa + 4352 + i * 272, lv[i]);
  CBAR();
  f32x4 d0 = {0, 0, 0, 0}, d1 = d0, d2 = d0;
#pragma unroll
  for (int ks = 0; ks < 4; ++ks) {
    const u32x4 bfr = lds_r128(ra + ks * 64);
    d0 = mfma16(lds_r128(ra + 4352 + ks * 64), bfr, d0);
    d1 = mfma16(lds_r128(ra + 8704 + ks * 64), bfr, d1);
    d2 = mfma16(lds_r128(ra + 13056 + ks * 64), bfr, d2);
  }
  CBAR();
  STAMP(17);
  d0[0] += ds0;
#define TW(a, v) *(LDS float*)(size_t)(a) = (v)
#pragma unroll
  for (int q = 0; q < 4; ++q) TW(t0a + 8 * q, d0[q]);
  TW(t1a, d1[0]); TW(t1a + 8, d1[1]); TW(t1b, d1[2]); TW(t1b + 8, d1[3]);
  if (lane <= 32) TW(t2a, d2[0]);
  if (g < 2) {
#pragma unroll
    for (int q = 1; q < 4; ++q) TW(t2a + 128 * q, d2[q]);
  }
#undef TW
  lds_flag_set(F_GP + gw * 4);
  STAMP(18);
  lds_wait<1, 0>(F_GP + (gw ^ 1) * 4);
  STAMP(19);
  f32x2 tp[19];
  float ta[19];
#pragma unroll
  for (int i = 0; i < 19; ++i) tp[i] = *(LDS f32x2*)(size_t)(i < 16 ? ea[i] : tab + (512 - (i - 16)) * 8);
  SB();
#pragma unroll
  for (int i = 0; i < 19; ++i) ta[i] = fadd1(tp[i][0], tp[i][1]);
#pragma unroll
  for (int it = 0; it < 2; ++it)
#pragma unroll
    for (int e = 0; e < 4; ++e) kv[it][e] = pack_bf16(ta[it * 8 + 2 * e], ta[it * 8 + 2 * e + 1]);
  {
    const float a = ta[16], b = ta[17], c = ta[18];
    const u32x4 nx = hw ? u32x4{0, 0, 0, 0} : u32x4{pack_bf16(a, a), pack_bf16(b, b), pack_bf16(b, c), pack_bf16(c, c)};
#pragma unroll
    for (int e = 0; e < 4; ++e) {
      kv[2][e] = __builtin_amdgcn_update_dpp(0u, lane == 0 ? kv[1][e] : kv[0][e], 0x134, 0xf, 0xf, false);
      kv[3][e] = __builtin_amdgcn_update_dpp(0u, lane == 0 ? nx[e] : kv[1][e], 0x134, 0xf, 0xf, false);
    }
  }
  asm volatile("" : "+v"(kv[0]), "+v"(kv[1]), "+v"(kv[2]), "+v"(kv[3])::"memory");
  STAMP(2);
  return tag;
}

DEV float max3(float a, float b, float c) { return __builtin_fmaxf(__builtin_fmaxf(a, b), c); }
DEV float rowmax16(f32x16 s) {
  return max3(max3(max3(s[0], s[1], s[2]), max3(s[3], s[4], s[5]), max3(s[6], s[7], s[8])),
              max3(max3(s[9], s[10], s[11]), max3(s[12], s[13], s[14]), s[15]), s[15]);
}
DEV float halfmax(float m) {
  auto r = __builtin_amdgcn_permlane32_swap(f2u(m), f2u(m), false, false);
  return __builtin_fmaxf(u2f(r[0]), u2f(r[1]));
}
DEV f32x8 lo8(f32x16 v) { return __builtin_shufflevector(v, v, 0, 1, 2, 3, 4, 5, 6, 7); }
DEV f32x8 hi8(f32x16 v) { return __builtin_shufflevector(v, v, 8, 9, 10, 11, 12, 13, 14, 15); }
#define EXPS(S, a, b) _Pragma("unroll") for (int i_ = a; i_ < b; ++i_) { float e_ = S[i_]; asm volatile("v_exp_f32 %0, %0" : "+v"(e_)); S[i_] = e_; }
#define PV(O, V, P8) asm volatile("v_smfmac_f32_32x32x32_bf16 %0, %1, %2, %3" : "+v"(O) : "v"(V), "v"(P8), "v"(idx))
#define QK(D, Q, NM) asm volatile("v_mfma_f32_32x32x16_bf16 %0, %1, %2, %3" : "=v"(D) : "v"(kn), "v"(Q), "v"(NM))
#define TILE(T, VL, VH, XL, XH)                                    \
  EXPS(SA, 0, 2); SB();                                            \
  PV(OB, XH, hi8(SBv)); SB();                                      \
  EXPS(SA, 2, 6); SB();                                            \
  QK(SBv, qB, nmB); SB();                                          \
  kn = lds_r128(kb + ((T) + 1) * KVS); SB();                       \
  EXPS(SA, 6, 11); SB();                                           \
  PV(OA, VL, lo8(SA)); SB();                                       \
  EXPS(SA, 11, 16); SB();                                          \
  EXPS(SBv, 0, 2); SB();                                           \
  PV(OA, VH, hi8(SA)); SB();                                       \
  XL = lds_r128(vb0 + ((T) + 1) * KVS); SB();                      \
  EXPS(SBv, 2, 6); SB();                                           \
  QK(SA, qA, nmA); SB();                                           \
  XH = lds_r128(vb1 + ((T) + 1) * KVS); SB();                      \
  EXPS(SBv, 6, 11); SB();                                          \
  PV(OB, VL, lo8(SBv)); SB();                                      \
  EXPS(SBv, 11, 16); SB();

DEV void attn_exact(const char* kp, int t0, int t1, u32x4 q, f32x16& O, float& m, int lane) {
  const int r = lane & 31, idx = (int)0xDDDDDDDDu;
  const f32x16 zero16 = {0, 0, 0, 0, 0, 0, 0, 0, 0, 0, 0, 0, 0, 0, 0, 0};
  const unsigned one = (r == 16 || r == 20) ? 0x3f803f80u : 0u, vo = (WS_VF - WS_KF) + (lane >> 5) * 256 + (r & 15) * 16;
  float mm = -__builtin_inff();
  O = zero16;
#pragma clang loop unroll(disable)
  for (int pass = 0; pass < 2; ++pass) {
#pragma clang loop unroll(disable)
    for (int t = t0; t < t1; ++t) {
      u32x4 k, v0, v1;
      asm volatile("global_load_dwordx4 %0, %3, %5 sc1\n\tglobal_load_dwordx4 %1, %4, %5 sc1\n\tglobal_load_dwordx4 %2, %4, %5 offset:512 sc1\n\t"
                   "s_waitcnt vmcnt(0)" : "=&v"(k), "=&v"(v0), "=&v"(v1) : "v"(t * 1024 + lane * 16), "v"(t * 1024 + vo), "s"(kp) : "memory");
      f32x16 s = mfma32(k, q, zero16);
      if (pass == 0) { mm = __builtin_fmaxf(mm, rowmax16(s)); continue; }
      if (r >= 16) v0 = v1 = u32x4{one, one, one, one};
#pragma unroll
      for (int i = 0; i < 16; ++i) s[i] = __builtin_amdgcn_exp2f(s[i] - mm);
      O = __builtin_amdgcn_smfmac_f32_32x32x32_bf16(as_bf8(v0), __builtin_bit_cast(bf16x16, lo8(s)), O, idx, 0, 0);
      O = __builtin_amdgcn_smfmac_f32_32x32x32_bf16(as_bf8(v1), __builtin_bit_cast(bf16x16, hi8(s)), O, idx, 0, 0);
    }
    if (pass == 0) mm = halfmax(mm);
  }
  m = mm;
}

DEV void b_attn(char* ws, int bid, int w, int lane PROF_ARG) {
  const int head = 4 * (bid & 7) + ((bid >> 3) & 3), qt = (bid >> 5) * 8 + 2 * (w & 3), kh = w >> 2, t0 = kh * NKA;
  const int r = lane & 31, h = lane >> 5;
  const u32x2 tag = get_tag();
  unsigned kb = t0 * KVS + lane * 16;
  const unsigned oth = (r == 16 || r == 20) ? 2048 : 2064;
  unsigned vb0 = t0 * KVS + (r < 16 ? 1024 + h * 256 + r * 16 : oth);
  unsigned vb1 = t0 * KVS + (r < 16 ? 1536 + h * 256 + r * 16 : oth);
  int idx = (int)0xDDDDDDDDu;
  f32x16 OA, OB, SBv;
#pragma unroll
  for (int i = 0; i < 16; ++i) { OA[i] = 0.f; OB[i] = 0.f; SBv[i] = 0.f; }
  u32x4 xh = u32x4{0, 0, 0, 0};
  asm volatile("" : "+v"(SBv), "+v"(OA), "+v"(OB), "+v"(xh), "+v"(kb), "+v"(vb0), "+v"(vb1), "+v"(idx));
  STAMP(3);
  if (w == 0) {
    lds_wait<4, 2>(F_GD);
    const unsigned v = (lane & 1) ? 0u : 0x3f803f80u;
    lds_w128(((lane >> 1) + (lane < 32 ? 0 : NKA - 16)) * KVS + 2048 + (lane & 1) * 16, u32x4{v, v, v, v});
    lds_flag_set(F_ONES);
  }
  if (w == 4) {
    const int cq = (head >> 1) / 3, ck = (16 + (head >> 1)) / 3, cv = (32 + (head >> 1)) / 3;
    const int g = lane >> 4, i = lane & 15;
    poll1(ws, WS_FA + (g == 0 ? ck * 16 + i : g == 1 ? cv * 16 + i : cq * 16 + 2 * (bid >> 5) + (i & 1)) * 128, tag);
    lds_wait<1, 0>(F_ONES);
    lds_flag_set(F_GO);
  } else {
    if (w < 4) lds_wait<4, 4>(F_PD);
    lds_wait<1, SL1>(F_GO);
  }
  STAMP(4);
  PRIO(2);
  u32x4 qA, qB;
  asm volatile("global_load_dwordx4 %0, %2, %3 sc1\n\tglobal_load_dwordx4 %1, %2, %3 offset:1024 sc1"
               : "=&v"(qA), "=&v"(qB) : "v"(lane * 16), "s"(ws + WS_QF + (head * 64 + qt) * 1024) : "memory");
  const char* kp = ws + WS_KF + head * 65536;
  auto fetch = [&](int t) { dma16_dev(t * KVS, lane * 16, kp + t * 1024); dma16_dev(t * KVS + 1024, lane * 16, kp + (WS_VF - WS_KF) + t * 1024); };
  const int tw = t0 + (w & 3);
  fetch(tw); fetch(tw + 4);
  asm volatile("s_waitcnt vmcnt(3)" : "+v"(qA), "+v"(qB)::"memory");
  STAMP(10);
  const f32x16 zero16 = {0, 0, 0, 0, 0, 0, 0, 0, 0, 0, 0, 0, 0, 0, 0, 0};
  u32x4 kn = lds_r128(tw * KVS + lane * 16);
  float mA = halfmax(rowmax16(mfma32(kn, qA, zero16))), mB = halfmax(rowmax16(mfma32(kn, qB, zero16)));
  f32x16 nmA, nmB, SA;
#pragma unroll
  for (int i = 0; i < 16; ++i) { nmA[i] = -mA; nmB[i] = -mB; }
  asm volatile("" : "+v"(nmA), "+v"(nmB));
  STAMP(11);
  WAIT_VM(0);
  lds_flag_set(F_F0 + w * 4);
  lds_wait<4, 0>(F_F0 + kh * 16);
  STAMP(5); RSTAMP(26);
  kn = lds_r128(kb);
  QK(SA, qA, nmA);
  u32x4 vl = lds_r128(vb0), vh = lds_r128(vb1), xl;
  fetch(tw + 8); fetch(tw + 12);
  asm volatile("s_nop 7" : "+v"(SA));
#define PA0 6
#define PB0 5
  unsigned ev = 1u | (2u << 3) | (4u << 6) | (3u << 9) | (kh ? (PB0 < 6 ? 5u << (3 * PB0) : 0u) | (7u << 18) : (5u << (3 * PA0)) | (6u << 21));
#pragma clang loop unroll(disable)
  for (int g = kh ? (64 - NKA) / 4 : NKA / 4; g > 0; --g) {
    asm volatile("" : "+s"(ev));
    TILE(0, vl, vh, xl, xh)
    TILE(1, xl, xh, vl, vh)
    TILE(2, vl, vh, xl, xh)
    const unsigned e = ev & 7;
    if (e) {
      if (e == 1) { WAIT_VM(0); lds_flag_set(F_F1 + w * 4); }
      else if (e == 2) lds_wait<4, 0>(F_F1 + kh * 16);
      else if (e == 3) lds_wait<4, 0>(F_FS);
      else if (e == 4) PRIO(1);
      else if (e == 5) PRIO(0);
      else if (e == 6) lds_flag_set(F_AD + 16 + w * 4);
      else lds_wait<4, 0>(F_AD + 16);
    }
    TILE(3, xl, xh, vl, vh)
    kb += 4 * KVS; vb0 += 4 * KVS; vb1 += 4 * KVS;
    ev >>= 3;
  }
  asm volatile("s_nop 1");
  PV(OB, xh, hi8(SBv));
  asm volatile("s_nop 15" : "+v"(OA), "+v"(OB));
  if (__builtin_expect(__any(!(OA[8] <= 1073741824.f)), 0)) attn_exact(kp, t0, kh ? 64 : NKA, qA, OA, mA, lane);
  if (__builtin_expect(__any(!(OB[8] <= 1073741824.f)), 0)) attn_exact(kp, t0, kh ? 64 : NKA, qB, OB, mB, lane);
  STAMP(6); RSTAMP(27);
  if (kh) PRIO(3); else lds_wait<4, 0>(F_AD + 16);
  {
    const f32x16 X = kh ? OA : OB;
    const unsigned ex = B_EX + w * 2560 + lane * 16;
    *(LDS f32x4*)(size_t)ex = f32x4{X[0], X[1], X[2], X[3]};
    *(LDS f32x4*)(size_t)(ex + 1024) = f32x4{X[4], X[5], X[6], X[7]};
    *(LDS f32x2*)(size_t)(B_EX + w * 2560 + 2048 + lane * 8) = f32x2{X[8], kh ? mA : mB};
  }
  lds_flag_set(F_EX + w * 4);
  lds_wait<1, 1>(F_EX + (w ^ 4) * 4);
  PRIO(3);
  {
    const f32x16 X = kh ? OB : OA;
    const float m = kh ? mB : mA;
    const unsigned ex = B_EX + (w ^ 4) * 2560 + lane * 16;
    const f32x4 y0 = *(LDS f32x4*)(size_t)ex, y1 = *(LDS f32x4*)(size_t)(ex + 1024);
    const f32x2 y2 = *(LDS f32x2*)(size_t)(B_EX + (w ^ 4) * 2560 + 2048 + lane * 8);
    const float M = __builtin_fmaxf(m, y2[1]);
    float sx = __builtin_amdgcn_exp2f(m - M), sy = __builtin_amdgcn_exp2f(y2[1] - M);
    const float inv = __builtin_amdgcn_rcpf(__builtin_fmaf(X[8], sx, y2[0] * sy));
    sx *= inv; sy *= inv;
    unsigned ov[4];
#pragma unroll
    for (int j = 0; j < 2; ++j) {
      ov[j] = pack_bf16(__builtin_fmaf(X[2 * j], sx, y0[2 * j] * sy), __builtin_fmaf(X[2 * j + 1], sx, y0[2 * j + 1] * sy));
      ov[2 + j] = pack_bf16(__builtin_fmaf(X[4 + 2 * j], sx, y1[2 * j] * sy), __builtin_fmaf(X[5 + 2 * j], sx, y1[2 * j + 1] * sy));
    }
    st16_dev(ws, WS_QF + (head * 64 + qt + kh) * 1024 + lane * 16, u32x4{ov[0], ov[1], ov[2], ov[3]});
  }
  STAMP(12);
  publish(ws, WS_FO + ((qt + kh) * 32 + head) * 128, tag, lane);
  STAMP(7);
  const int T = (bid & 7) + 8 * ((bid >> 3) & 7);
  poll1(ws, WS_FO + (T * 32 + 4 * w + (lane & 3)) * 128, tag);
  STAMP(8);
#pragma unroll
  for (int i = 0; i < 4; ++i) dma16_dev(C_O + (4 * w + i) * 1024, lane * 16, ws + WS_QF + ((4 * w + i) * 64 + T) * 1024);
  WAIT_VM(0);
  lds_flag_set(F_OC + w * 4);
  STAMP(9); RSTAMP(28);
  if (bid == 0 && w == 0) {
    poll2(ws, WS_FY + lane * 128, WS_FY + (64 + lane) * 128, tag);
    poll2(ws, WS_FY + (128 + lane) * 128, WS_FY + (192 + lane) * 128, tag);
    if (lane == 0) st8_dev(ws, WS_EP, tag);
  }
}

#define MFMA16(D, A, B) asm volatile("v_mfma_f32_16x16x32_bf16 %0, %1, %2, %0" : "+v"(D) : "v"(A), "v"(B))
template <int CA, int CB>
DEV void conv_tiles(unsigned ka, char* ws, unsigned yo, unsigned ub) {
  f32x4 a0 = {0, 0, 0, 0}, a1 = a0, b0 = a0, b1 = a0;
  constexpr int NA = 8 * (CA + 2), NB = 8 * (CB + 2), LA = 4;
  u32x4 GA[8 + LA + 1], GB[8 + LA + 1], A[LA + 1];
  auto ga = [&](int q) { if (q < NA) GA[q % (8 + LA + 1)] = lds_r128(ub + 512 * (CA + 1) + 4096 - 64 * q); };
  auto gb = [&](int q) { if (q < NB) GB[q % (8 + LA + 1)] = lds_r128(ub + 512 * (CB + 1) + 4096 - 64 * q); };
  auto ra = [&](int q) { if (q < NB) A[q % (LA + 1)] = lds_r128(ka + 4032 - 64 * q); };
#pragma unroll
  for (int q = 0; q < LA; ++q) ra(q);
#pragma unroll
  for (int q = 0; q < 8 + LA; ++q) { gb(q); ga(q); }
  asm volatile("" : "+v"(a0), "+v"(a1), "+v"(b0), "+v"(b1));
#pragma unroll
  for (int s = 0; s < NB; ++s) {
    SB();
    MFMA16(b1, A[s % (LA + 1)], GB[s % (8 + LA + 1)]);
    SB();
    ra(s + LA);
    SB();
    if (s + 8 < NB) MFMA16(b0, A[s % (LA + 1)], GB[(s + 8) % (8 + LA + 1)]);
    SB();
    gb(s + 8 + LA);
    SB();
    if (s < NA) MFMA16(a1, A[s % (LA + 1)], GA[s % (8 + LA + 1)]);
    SB();
    ga(s + 8 + LA);
    SB();
    if (s + 8 < NA) MFMA16(a0, A[s % (LA + 1)], GA[(s + 8) % (8 + LA + 1)]);
  }
  asm volatile("s_nop 7" : "+v"(a0), "+v"(a1), "+v"(b0), "+v"(b1));
  st8_dev(ws, yo + 262144 * CA, u32x2{pack_bf16(a0[0], a0[1]), pack_bf16(a0[2], a0[3])});
  st8_dev(ws, yo + 262144 * (CA + 1), u32x2{pack_bf16(a1[0], a1[1]), pack_bf16(a1[2], a1[3])});
  st8_dev(ws, yo + 262144 * CB, u32x2{pack_bf16(b0[0], b0[1]), pack_bf16(b0[2], b0[3])});
  st8_dev(ws, yo + 262144 * (CB + 1), u32x2{pack_bf16(b1[0], b1[1]), pack_bf16(b1[2], b1[3])});
}

DEV void s_rest(const Params& P, u32x2 tag, int bid, int cw, int lane, const u32x4 (&kv)[4] PROF_ARG) {
  char* ws = P.ws;
  const int head = 4 * (bid & 7) + ((bid >> 3) & 3);
  const int d = 2 * bid + (cw >> 1), hw = cw & 1;
  {
#define CPRIO 1
    PRIO(CPRIO);
    const unsigned ur = B_U + (cw >> 1) * 4608;
    const unsigned tb = conv_region(cw >> 1), ta = tb + (128 * hw + lane) * 16;
    unsigned Od[2][7];
#pragma unroll
    for (int it = 0; it < 2; ++it)
#pragma unroll
      for (int k = 0; k < 7; ++k) {
        Od[it][k] = __builtin_amdgcn_alignbit(k < 3 ? kv[it][k + 1] : kv[2 + it][k - 3], k < 4 ? kv[it][k] : kv[2 + it][k - 4], 16);
        asm volatile("" : "+v"(Od[it][k]));
      }
    STAMP(20);
    if (hw == 1 && lane < 32) lds_w128(tb + (lane >> 2) * 4160 + 4096 + (lane & 3) * 16, u32x4{0, 0, 0, 0});
#pragma unroll
    for (int it = 0; it < 2; ++it) {
      const unsigned D[8] = {kv[it][0], kv[it][1], kv[it][2], kv[it][3], kv[2 + it][0], kv[2 + it][1], kv[2 + it][2], kv[2 + it][3]};
#pragma unroll
      for (int e = 0; e < 8; ++e) {
        const int k = e >> 1;
        lds_w128(ta + it * 1024 + e * 4160,
                 (e & 1) ? u32x4{Od[it][k], Od[it][k + 1], Od[it][k + 2], Od[it][k + 3]} : u32x4{D[k], D[k + 1], D[k + 2], D[k + 3]});
      }
    }
    lds_flag_set(F_T1 + cw * 4);
    STAMP(21);
    lds_wait<1, 0>(F_XGO);
    lds_wait<1, 0>(F_T1 + (cw ^ 1) * 4);
    STAMP(4);
    const int j = lane & 15, g = lane >> 4;
    unsigned ka = tb + ((7 - j) & 7) * 4160 + (2024 + 8 * g - 8 * (j >> 3)) * 2 - 4032;
    unsigned ub = ur + 512 - 32 + 32 * j + 16 * g - 4096;
    asm volatile("" : "+v"(ka), "+v"(ub));
    const unsigned yo = WS_XT + ((j >> 1) * 512 + d) * 64 + (16 * (j & 1) + 4 * g) * 2;
    if (hw) conv_tiles<2, 4>(ka, ws, yo, ub);
    else conv_tiles<0, 6>(ka, ws, yo, ub);
    lds_flag_set(F_CD + cw * 4);
    STAMP(5);
    lds_wait<4, 0>(F_CD);
    lds_wait<8, 1>(F_F0);
    const char* kp = ws + WS_KF + head * 65536;
#pragma unroll
    for (int i = 0; i < 8; ++i) {
      const int n = 4 * i + cw, t = n < NKA - 16 ? 16 + n : 32 + n;
      dma16_dev(t * KVS, lane * 16, kp + t * 1024);
      dma16_dev(t * KVS + 1024, lane * 16, kp + (WS_VF - WS_KF) + t * 1024);
    }
    if (lane < 16) {
      const int n = 4 * (lane >> 1) + cw, t = n < NKA - 16 ? 16 + n : 32 + n;
      const unsigned v = (lane & 1) ? 0u : 0x3f803f80u;
      lds_w128(t * KVS + 2048 + (lane & 1) * 16, u32x4{v, v, v, v});
    }
    WAIT_VM(0);
    lds_flag_set(F_FS + cw * 4);
    STAMP(6);
    PRIO(3);
    publish(ws, WS_FY + bid * 128 + cw * 8, tag, lane);
  }
  const int T = (bid & 7) + 8 * ((bid >> 3) & 7), dt = 4 * (bid >> 6) + cw;
  const int g = lane >> 4, q4 = (lane & 15) >> 2, pp = lane & 3;
  const unsigned st = C_ST + cw * 7424;
  const unsigned wa = st + (4 * (g >> 1) + q4) * 64 + (g & 1) * 32 + pp * 8;
  unsigned vw[4];
#pragma unroll
  for (int i = 0; i < 4; ++i) vw[i] = (lane >> 2) * 1024 + (lane & 3) * 16 + i * (16384 - 1024);
  u32x4 wf[28];
#pragma unroll
  for (int m = 0; m < 8; ++m) {
    dma16x4(st, vw, P.wo + dt * 64 + m * 65536);
    if (m == 7) break;
    WAIT_VM(0);
#pragma unroll
    for (int i = 0; i < 4; ++i) {
      const u32x2 lo = lds_tr(wa + i * 1024), hi = lds_tr(wa + i * 1024 + 512);
      wf[4 * m + i] = u32x4{lo[0], lo[1], hi[0], hi[1]};
    }
    asm volatile("s_waitcnt lgkmcnt(0)" : "+v"(wf[4 * m]), "+v"(wf[4 * m + 1]), "+v"(wf[4 * m + 2]), "+v"(wf[4 * m + 3])::"memory");
  }
  STAMP(7);
  const int tl = lane & 31, h = lane >> 5, t = 32 * T + tl;
  poll2(ws, WS_FY + (16 * dt + (lane >> 2)) * 128 + (lane & 3) * 8, WS_FY + (4 * T + ((lane >> 2) & 3)) * 128 + (lane & 3) * 8, tag);
  dma16_dev(st + 4096, lane * 16, ws + WS_XT + (T * 512 + 32 * dt) * 64);
  dma16_dev(st + 5120, lane * 16, ws + WS_XT + (T * 512 + 32 * dt) * 64 + 1024);
  float gt;
  asm volatile("global_load_dword %0, %1, %2 sc1\n\ts_waitcnt vmcnt(0)" : "=&v"(gt) : "v"(t * 4), "s"(ws + WS_G) : "memory");
  f32x16 acc;
#pragma unroll
  for (int i = 0; i < 16; ++i) acc[i] = 0.f;
  asm volatile("" : "+v"(acc));
  STAMP(8);
  const unsigned oa = C_O + lane * 16;
  PRIO(0); lds_wait<1, 8>(F_EX + 28); PRIO(3);
  lds_wait<8, SL1>(F_OC);
  STAMP(11);
  u32x4 of[3], wl[2];
  u32x2 y[4];
  float yb[16];
  const float omg = 1.0f - gt;
  of[0] = lds_r128(oa); of[1] = lds_r128(oa + 1024);
#pragma unroll
  for (int m = 0; m < 4; ++m) y[m] = lds_tr(st + 4096 + (8 * m + 4 * (g >> 1) + q4) * 64 + (g & 1) * 32 + pp * 8);
#pragma unroll
  for (int k = 0; k < 32; ++k) {
    SB();
    if (k < 28) MFMA32(acc, wf[k], of[k % 3]); else MFMA32(acc, wl[k & 1], of[k % 3]);
    SB();
    if (k < 30) of[(k + 2) % 3] = lds_r128(oa + (k + 2) * 1024);
    if (k >= 26 && k < 30) {
      const u32x2 lo = lds_tr(wa + (k - 26) * 1024), hi = lds_tr(wa + (k - 26) * 1024 + 512);
      wl[k & 1] = u32x4{lo[0], lo[1], hi[0], hi[1]};
    }
    if (k >= 8 && k < 16) {
      yb[2 * (k - 8)] = omg * bf_lo(y[(k - 8) >> 1][k & 1]); yb[2 * (k - 8) + 1] = omg * bf_hi(y[(k - 8) >> 1][k & 1]);
      asm volatile("" : "+v"(yb[2 * (k - 8)]), "+v"(yb[2 * (k - 8) + 1]));
    }
  }
  asm volatile("s_nop 15" : "+v"(acc));
  STAMP(10);
#pragma unroll
  for (int m = 0; m < 4; m += 2) {
    u32x2 a, b;
#pragma unroll
    for (int e = 0; e < 2; ++e) {
      a[e] = pack_bf16(__builtin_fmaf(gt, acc[4 * m + 2 * e], yb[4 * m + 2 * e]), __builtin_fmaf(gt, acc[4 * m + 2 * e + 1], yb[4 * m + 2 * e + 1]));
      b[e] = pack_bf16(__builtin_fmaf(gt, acc[4 * m + 4 + 2 * e], yb[4 * m + 4 + 2 * e]), __builtin_fmaf(gt, acc[4 * m + 5 + 2 * e], yb[4 * m + 5 + 2 * e]));
    }
    auto s0 = __builtin_amdgcn_permlane32_swap(a[0], b[0], false, false);
    auto s1 = __builtin_amdgcn_permlane32_swap(a[1], b[1], false, false);
    const unsigned r0 = s0[0], r1 = s1[0], r2 = s0[1], r3 = s1[1];
    *(u32x4*)(P.out + (size_t)(t * 1024 + (32 * dt + 8 * m + 8 * h) * 2)) = u32x4{r0, r1, r2, r3};
  }
}

__global__ __launch_bounds__(768, 3) void gated_mix_kernel(Params P) {
  const int bid = blockIdx.x, lane = threadIdx.x & 63;
  const int w = __builtin_amdgcn_readfirstlane(threadIdx.x >> 6);
  PROF_DECL;
  if (threadIdx.x < 32) asm volatile("ds_write_b128 %0, %1\n\ts_waitcnt lgkmcnt(0)" ::"v"(FL + lane * 16), "v"(u32x4{0, 0, 0, 0}) : "memory");
  __builtin_amdgcn_s_barrier();
  if (__builtin_expect(w < 8, 1)) {
    const int xcd = bid & 7, j = bid >> 3;
    const int tb = 4 * (xcd >> 1) + (j & 3), c = 8 * (xcd & 1) + (j >> 2);
    const int s0 = (2 * (xcd >> 1) + 4 * (xcd & 1)) & 7;
    if (__builtin_expect(w < 4, 1)) {
      a_load(P.x, P.wqkv, P.ws, bid, tb, c, s0, w, lane PROF_PASS);
    } else {
      PRIO(3);
      if (c < 10) a_gemm<0>(P.ws, P.x, P.wqkv, tb, c, s0, w - 4, lane PROF_PASS);
      else if (c == 10) a_gemm<1>(P.ws, P.x, P.wqkv, tb, c, s0, w - 4, lane PROF_PASS);
      else a_gemm<3>(P.ws, P.x, P.wqkv, tb, c, s0, w - 4, lane PROF_PASS);
    }
    b_attn(P.ws, bid, w, lane PROF_PASS);
  } else {
    PRIO(3);
    u32x4 kv[4];
    const u32x2 tag = s_gen(P, bid, w - 8, lane, kv PROF_PASS);
    s_rest(P, tag, bid, w - 8, lane, kv PROF_PASS);
  }
  PROF_END(0, P.ws);
}

extern "C" void kernel_launch(void* const* d_in, const int* in_sizes, int n_in, void* d_out, int out_size, void* d_ws,
                              size_t ws_size, hipStream_t stream) {
  (void)in_sizes; (void)n_in; (void)out_size; (void)ws_size;
  (void)hipFuncSetAttribute((const void*)gated_mix_kernel, hipFuncAttributeMaxDynamicSharedMemorySize, LDS_BYTES);
  Params P;
  P.x = (const char*)d_in[0]; P.wqkv = (const char*)d_in[1]; P.wo = (const char*)d_in[2];
  P.Are = (const char*)d_in[4]; P.Aim = (const char*)d_in[5]; P.Bre = (const char*)d_in[6]; P.Bim = (const char*)d_in[7];
  P.Cre = (const char*)d_in[8]; P.Cim = (const char*)d_in[9]; P.logdt = (const char*)d_in[10]; P.dskip = (const char*)d_in[11];
  P.gatew = (const char*)d_in[12]; P.ws = (char*)d_ws; P.out = (char*)d_out;
  gated_mix_kernel<<<256, 768, LDS_BYTES, stream>>>(P);
}
```

```cpp
#include <hip/hip_runtime.h>
#include <hip/hip_bf16.h>
#include <stdint.h>

typedef __attribute__((ext_vector_type(16))) float f32x16;
typedef __attribute__((ext_vector_type(8))) float f32x8;
typedef __attribute__((ext_vector_type(4))) float f32x4;
typedef __attribute__((ext_vector_type(2))) float f32x2;
typedef __attribute__((ext_vector_type(2))) __bf16 bf16x2;
typedef __attribute__((ext_vector_type(8))) __bf16 bf16x8;
typedef __attribute__((ext_vector_type(16))) __bf16 bf16x16;
typedef __attribute__((ext_vector_type(4))) short s16x4;
typedef __attribute__((ext_vector_type(4))) unsigned u32x4;
typedef __attribute__((ext_vector_type(2))) unsigned u32x2;
#define LDS __attribute__((address_space(3)))
#define DEV __device__ __forceinline__

constexpr unsigned WS_QF = 0;
constexpr unsigned WS_KF = 2u << 20;
constexpr unsigned WS_VF = 4u << 20;
constexpr unsigned WS_XT = 6u << 20;
constexpr unsigned WS_G  = 8u << 20;
constexpr unsigned WS_EP = WS_G + 8192;
constexpr unsigned WS_FA = WS_G + 16384;
constexpr unsigned WS_FX = WS_FA + 32768;
constexpr unsigned WS_FY = WS_FX + 32768;
constexpr unsigned WS_FO = WS_FY + 32768;
#define NKERN 1
#define F3_LAYOUT

constexpr unsigned A_W = 0;
constexpr unsigned A_X = 61440;
constexpr unsigned B_U = 143360;
constexpr unsigned B_TAB = 152576;
DEV unsigned conv_region(int p) { return p ? 108160u : 33280u; }
constexpr unsigned KVS = 2080;
constexpr int NKA = 36;
constexpr unsigned B_EX = 0;
constexpr unsigned C_O = 20480;
constexpr unsigned C_ST = 133376;
constexpr unsigned FL = 163328;
constexpr unsigned F_LANDED = FL,
                   F_EX = FL + 32,
                   F_DONE = FL + 64,
                   F_ONES = FL + 112, F_XD = FL + 128, F_XGO = FL + 132, F_GO = FL + 136, F_TAGOK = FL + 140,
                   F_GP = FL + 144,
                   F_GD = FL + 176,
                   F_F0 = FL + 192, F_F1 = FL + 224, F_F2 = FL + 416, F_FS = FL + 256,
                   F_CD = FL + 272,
                   F_AD = FL + 288,
                   F_OC = FL + 320,
                   F_TAG = FL + 352, F_PD = FL + 368, F_T1 = FL + 384;
constexpr unsigned LDS_BYTES = 163840;

#define PROF_DECL
#define STAMP(i)
#define STAMPN(i)
#define RSTAMP(i)
#define PROF_ARG
#define PROF_PASS
#define PROF_END(kid, ws_)

DEV unsigned pack_bf16(float lo, float hi) {
  f32x2 a = {lo, hi};
  return __builtin_bit_cast(unsigned, __builtin_convertvector(a, bf16x2));
}
DEV float u2f(unsigned u) { return __builtin_bit_cast(float, u); }
DEV unsigned f2u(float f) { return __builtin_bit_cast(unsigned, f); }
DEV float bf_lo(unsigned u) { return u2f(u << 16); }
DEV float bf_hi(unsigned u) { return u2f(u & 0xffff0000u); }
DEV float round_bf16(float f) { return bf_lo(pack_bf16(f, f)); }

#define WAIT_VM(n) asm volatile("s_waitcnt vmcnt(" #n ")" ::: "memory")
#define CBAR() asm volatile("" ::: "memory")
#define SB() __builtin_amdgcn_sched_barrier(0)
#define PRIO(n) __builtin_amdgcn_s_setprio(n)
DEV void dma16_dev(unsigned lds_addr, unsigned voff, const void* sbase) {
  asm volatile("s_mov_b32 m0, %0\n\ts_nop 0\n\tglobal_load_lds_dwordx4 %1, %2 sc1" ::"s"(lds_addr), "v"(voff), "s"(sbase) : "memory");
}
DEV void dma16x4(unsigned lds_addr, const unsigned* v, const void* sbase) {
  asm volatile("s_mov_b32 m0, %0\n\ts_nop 0\n\tglobal_load_lds_dwordx4 %1, %5\n\tglobal_load_lds_dwordx4 %2, %5 offset:1024\n\t"
               "global_load_lds_dwordx4 %3, %5 offset:2048\n\tglobal_load_lds_dwordx4 %4, %5 offset:3072"
               ::"s"(lds_addr), "v"(v[0]), "v"(v[1]), "v"(v[2]), "v"(v[3]), "s"(sbase) : "memory");
}
DEV u32x4 lds_r128(unsigned a) { return *(LDS u32x4*)(size_t)a; }
DEV void lds_w128(unsigned a, u32x4 v) { *(LDS u32x4*)(size_t)a = v; }
DEV void lds_w32(unsigned a, unsigned v) { *(LDS unsigned*)(size_t)a = v; }
DEV u32x2 lds_tr(unsigned a) { return __builtin_bit_cast(u32x2, __builtin_amdgcn_ds_read_tr16_b64_v4i16((LDS s16x4*)(size_t)a)); }
DEV bf16x8 as_frag(u32x4 v) { return __builtin_bit_cast(bf16x8, v); }
DEV f32x16 mfma32(u32x4 a, u32x4 b, f32x16 c) { return __builtin_amdgcn_mfma_f32_32x32x16_bf16(as_frag(a), as_frag(b), c, 0, 0, 0); }
DEV f32x4 mfma16(u32x4 a, u32x4 b, f32x4 c) { return __builtin_amdgcn_mfma_f32_16x16x32_bf16(as_frag(a), as_frag(b), c, 0, 0, 0); }
#define MFMA32(D, A, B) asm volatile("v_mfma_f32_32x32x16_bf16 %0, %1, %2, %0" : "+v"(D) : "v"(A), "v"(B))
DEV void st16_dev(const void* base, unsigned off, u32x4 v) { asm volatile("global_store_dwordx4 %0, %1, %2 sc1\n\ts_nop 1" ::"v"(off), "v"(v), "s"(base) : "memory"); }
DEV void st8_dev(const void* base, unsigned off, u32x2 v) { asm volatile("global_store_dwordx2 %0, %1, %2 sc1" ::"v"(off), "v"(v), "s"(base) : "memory"); }
DEV void st4_dev(const void* base, unsigned off, unsigned v) { asm volatile("global_store_dword %0, %1, %2 sc1" ::"v"(off), "v"(v), "s"(base) : "memory"); }

DEV void lds_flag_set(unsigned a) { asm volatile("ds_write_b32 %0, %1" ::"v"(a), "v"(1u) : "memory"); }
template <int NW, int SLEEP> DEV void lds_wait(unsigned a) {
#pragma clang loop unroll(disable)
  for (int n = 0; n < (1 << 21); ++n) {
    unsigned ok;
    if (NW == 8) {
      u32x4 v, u;
      asm volatile("ds_read_b128 %0, %2\n\tds_read_b128 %1, %2 offset:16\n\ts_waitcnt lgkmcnt(0)" : "=&v"(v), "=&v"(u) : "v"(a) : "memory");
      ok = v[0] & v[1] & v[2] & v[3] & u[0] & u[1] & u[2] & u[3];
    } else if (NW == 4) {
      u32x4 v;
      asm volatile("ds_read_b128 %0, %1\n\ts_waitcnt lgkmcnt(0)" : "=&v"(v) : "v"(a) : "memory");
      ok = v[0] & v[1] & v[2] & v[3];
    } else {
      asm volatile("ds_read_b32 %0, %1\n\ts_waitcnt lgkmcnt(0)" : "=&v"(ok) : "v"(a) : "memory");
    }
    if (__builtin_amdgcn_readfirstlane(ok)) break;
    if (SLEEP) __builtin_amdgcn_s_sleep(SLEEP);
  }
}
DEV u32x2 get_tag() {
  lds_wait<1, 1>(F_TAGOK);
  return *(volatile LDS u32x2*)(size_t)F_TAG;
}
#define PSLEEP 4
DEV void publish(char* ws, unsigned off, u32x2 tag, int lane) {
  WAIT_VM(0);
  if (lane == 0) st8_dev(ws, off, tag);
}
DEV void poll1(const char* ws, unsigned off, u32x2 tag) {
#pragma clang loop unroll(disable)
  for (int n = 0; n < (1 << 17); ++n) {
    u32x2 a;
    asm volatile("global_load_dwordx2 %0, %1, %2 sc1\n\ts_waitcnt vmcnt(0)" : "=&v"(a) : "v"(off), "s"(ws) : "memory");
    if (!__any(a[0] != tag[0] || a[1] != tag[1])) break;
    __builtin_amdgcn_s_sleep(PSLEEP);
  }
}
DEV void poll1_atomic(const char* ws, unsigned off, u32x2 tag, bool on) {
#pragma clang loop unroll(disable)
  for (int n = 0; n < (1 << 17); ++n) {
    u32x2 a = tag;
    if (on) asm volatile("global_atomic_add_x2 %0, %1, %2, %3 sc0 sc1\n\ts_waitcnt vmcnt(0)" : "=&v"(a) : "v"(off), "v"(u32x2{0, 0}), "s"(ws) : "memory");
    on = a[0] != tag[0] || a[1] != tag[1];
    if (!__any(on)) break;
    __builtin_amdgcn_s_sleep(PSLEEP);
  }
}
DEV void poll2(const char* ws, unsigned off0, unsigned off1, u32x2 tag) {
#pragma clang loop unroll(disable)
  for (int n = 0; n < (1 << 17); ++n) {
    u32x2 a, b;
    asm volatile("global_load_dwordx2 %0, %2, %4 sc1\n\tglobal_load_dwordx2 %1, %3, %4 sc1\n\ts_waitcnt vmcnt(0)"
                 : "=&v"(a), "=&v"(b) : "v"(off0), "v"(off1), "s"(ws) : "memory");
    if (!__any(a[0] != tag[0] || a[1] != tag[1] || b[0] != tag[0] || b[1] != tag[1])) break;
    __builtin_amdgcn_s_sleep(PSLEEP);
  }
}

#define STAG 6
DEV void a_load(const char* x, const char* wq, char* ws, int bid, int tb, int c, int s0, int l, int lane PROF_ARG) {
  PRIO(2);
  unsigned vw[12], vx[16];
#pragma unroll
  for (int n = 0; n < 12; ++n) {
    const int m = 64 * (n % 3) + lane, row = m / 12;
    vw[n] = row * 3072 + (m - 12 * row) * 16 + (n / 3) * 49152 + 3072 - (n & 3) * 1024;
  }
#pragma unroll
  for (int n = 0; n < 16; ++n)
    vx[n] = (lane >> 3) * 1024 + (((lane & 7) ^ (4 * (n & 1) + (lane >> 4))) << 4) + (n >> 2) * 32768 + (n & 3) * 7168;
  wq += 192 * c - 3072;
  x += (size_t)(128 * tb) * 1024;
  for (int n = 0; n < l; ++n) __builtin_amdgcn_s_sleep(STAG);
  auto issue = [&](int i) {
    const int s = (s0 + i) & 7, sl = i % 5;
#pragma unroll
    for (int q = 0; q < 4; ++q) {
      dma16x4(A_X + q * 20480 + sl * 4096, vx + 4 * q, x + s * 128);
      if (q < 3) dma16x4(A_W + sl * 12288 + q * 4096, vw + 4 * q, wq + (size_t)s * (64 * 3072));
    }
  };
  STAMP(16);
  issue(l);
  STAMP(17);
  WAIT_VM(0);
  lds_flag_set(F_LANDED + l * 4);
  STAMP(18);
  if (l == 0) issue(4);
  const int ix = (c & 7) >> 1, xb = 2 * ((s0 + ix) & 7) + (c & 1);
  if (l == ix) {
    const int g = lane >> 4, q4 = (lane & 15) >> 2, pp = lane & 3;
    u32x2 t[4][4];
#pragma unroll
    for (int m = 0; m < 4; ++m) {
      const int row = 8 * g + 4 * (m & 1) + q4;
      const int ch = (4 * (c & 1) + 2 * (m >> 1) + (pp >> 1)) ^ ((row >> 1) & 7);
#pragma unroll
      for (int q = 0; q < 4; ++q) t[q][m] = lds_tr(A_X + l * 4096 + row * 128 + ch * 16 + (pp & 1) * 8 + q * 20480);
    }
    lds_flag_set(F_XD);
    const unsigned off = WS_XT + (4 * tb * 512 + 32 * xb + (lane & 15)) * 64 + g * 16;
#pragma unroll
    for (int q = 0; q < 4; ++q) {
      st16_dev(ws, off + q * 32768, u32x4{t[q][0][0], t[q][0][1], t[q][1][0], t[q][1][1]});
      st16_dev(ws, off + q * 32768 + 1024, u32x4{t[q][2][0], t[q][2][1], t[q][3][0], t[q][3][1]});
    }
  }
  if (l > 0) {
    lds_wait<4, 0>(F_DONE + (l - 1) * 16);
    if (l - 1 == ix) lds_wait<1, 0>(F_XD);
    STAMP(19);
    issue(l + 4);
  }
  STAMP(20);
  WAIT_VM(0);
  lds_flag_set(F_LANDED + (l + 4) * 4);
  STAMP(21);
  if (l == ix) {
    const u32x2 tag = get_tag();
    if (lane == 0) st8_dev(ws, WS_FX + (xb * 16 + tb) * 128, tag);
  }
  PRIO(0);
  if (l == 1) {
    poll1(ws, WS_FX + ((bid >> 4) * 16 + (lane & 15)) * 128, get_tag());
#pragma unroll
    for (int i = 0; i < 8; ++i)
      dma16_dev(B_U + (i >> 2) * 4608 + 512 + (i & 3) * 1024, (lane >> 2) * 32768 + (lane & 3) * 16,
                ws + WS_XT + (2 * bid + (i >> 2)) * 64 + (i & 3) * (16 * 32768));
    WAIT_VM(0);
    lds_flag_set(F_XGO);
    STAMP(22);
  }
}

template <int NV>
DEV void a_gemm(char* ws, int tb, int c, int w, int lane PROF_ARG) {
  const int r = lane & 31, h = lane >> 5;
  const unsigned xr = A_X + w * 20480;
  unsigned xa[4];
#pragma unroll
  for (int ks = 0; ks < 4; ++ks) { xa[ks] = xr + r * 128 + ((((h ^ (r >> 1)) & 7) << 4) ^ (ks << 5)); asm volatile("" : "+v"(xa[ks])); }
  const int g = lane >> 4, q4 = (lane & 15) >> 2, pp = lane & 3;
  unsigned wa = A_W + (8 * (g >> 1) + q4) * 192 + (g & 1) * 32 + pp * 8;
  asm volatile("" : "+v"(wa));
  const int T = 4 * tb + w;
  f32x16 acc[3];
#pragma unroll
  for (int p = 0; p < 3; ++p)
#pragma unroll
    for (int i = 0; i < 16; ++i) acc[p][i] = 0.f;
  asm volatile("" : "+v"(acc[0]), "+v"(acc[1]), "+v"(acc[2]));
  const u32x2 tag = get_tag();
  lds_wait<1, 0>(F_LANDED);
  STAMP(1); RSTAMP(24);
  constexpr int PF = 2, NB = PF + 1;
  u32x4 xf[NB];
  u32x2 wl[NB][3], wh[NB][3];
  auto rdx = [&](int k) { xf[k % NB] = lds_r128(xa[k & 3] + ((k >> 2) % 5) * 4096); };
  auto rdw = [&](int k, int p) {
    wl[k % NB][p] = lds_tr(wa + ((k >> 2) % 5) * 12288 + (k & 3) * 3072 + p * 64);
    wh[k % NB][p] = lds_tr(wa + ((k >> 2) % 5) * 12288 + (k & 3) * 3072 + p * 64 + 768);
  };
#pragma unroll
  for (int k = 0; k < PF; ++k) { rdx(k); rdw(k, 0); rdw(k, 1); rdw(k, 2); }
  unsigned pk = 0;
#pragma unroll
  for (int k = 0; k < 32; ++k) {
    const int i = k >> 2, ks = k & 3;
    if (ks == 0) { STAMPN(16 + i); }
    SB();
#pragma unroll
    for (int p = 0; p < 3; ++p) {
      const u32x4 wf = {wl[k % NB][p][0], wl[k % NB][p][1], wh[k % NB][p][0], wh[k % NB][p][1]};
      if (p >= 3 - NV) MFMA32(acc[p], xf[k % NB], wf); else MFMA32(acc[p], wf, xf[k % NB]);
      SB();
      if (ks == 4 - PF && p == 0 && i < 7) {
        if (__builtin_expect(!__builtin_amdgcn_readfirstlane(pk), 0)) lds_wait<1, 0>(F_LANDED + (i + 1) * 4);
        CBAR();
      }
      if (k + PF < 32) { if (p == 0) rdx(k + PF); rdw(k + PF, p); }
      if (ks == 3 - PF && p == 1 && i < 7) pk = *(volatile LDS unsigned*)(size_t)(F_LANDED + (i + 1) * 4);
      if (ks == 3 - PF && p == 2) {
        if (i < 3) lds_flag_set(F_DONE + i * 16 + w * 4);
        else if (i == 7) lds_flag_set(F_GD + w * 4);
      }
      SB();
    }
  }
  asm volatile("s_nop 15" : "+v"(acc[0]), "+v"(acc[1]), "+v"(acc[2]));
  STAMP(2); RSTAMP(25);
#pragma unroll
  for (int p = 0; p < 3; ++p) {
    const int G = 3 * c + p, hp = G & 15;
    if (p >= 3 - NV) {
      const unsigned off = WS_VF + ((2 * hp + (r >> 4)) * 64 + T) * 1024 + h * 256 + (r & 15) * 16;
#pragma unroll
      for (int s = 0; s < 2; ++s) {
        u32x4 v;
#pragma unroll
        for (int j = 0; j < 4; ++j) v[j] = pack_bf16(acc[p][8 * s + 2 * j], acc[p][8 * s + 2 * j + 1]);
        st16_dev(ws, off + s * 512, v);
      }
    } else {
      const bool isq = G < 16;
      const float sc = isq ? 0.36067376022224085f : 1.0f;
      const int slotr = isq ? r : ((r & 19) | ((r & 4) << 1) | ((r & 8) >> 1));
      const unsigned off = (isq ? WS_QF : WS_KF) + (2 * hp * 64 + T) * 1024 + h * 512 + slotr * 16;
#pragma unroll
      for (int s = 0; s < 2; ++s) {
        u32x4 v;
#pragma unroll
        for (int j = 0; j < 4; ++j) v[j] = pack_bf16(acc[p][8 * s + 2 * j] * sc, acc[p][8 * s + 2 * j + 1] * sc);
        st16_dev(ws, off + s * 65536, v);
      }
    }
  }
  WAIT_VM(0);
  lds_flag_set(F_PD + w * 4);
  if (w == 0) {
    lds_wait<4, 0>(F_PD);
    if (lane == 0) st8_dev(ws, WS_FA + (c * 16 + tb) * 128, tag);
  }
}

struct cf { float r, i; };
DEV cf cmul(cf a, cf b) {
  cf o;
  asm("v_mul_f32 %0, %3, %5\n\tv_mul_f32 %1, %3, %4\n\tv_fma_f32 %0, %2, %4, -%0\n\tv_fmac_f32 %1, %2, %5"
      : "=&v"(o.r), "=&v"(o.i) : "v"(a.r), "v"(a.i), "v"(b.r), "v"(b.i));
  return o;
}
DEV void cmul2(cf& r0, cf a0, cf b0, cf& r1, cf a1, cf b1) {
  cf o, q;
  asm("v_mul_f32 %0, %5, %7\n\tv_mul_f32 %1, %5, %6\n\tv_mul_f32 %2, %9, %11\n\tv_mul_f32 %3, %9, %10\n\t"
      "v_fma_f32 %0, %4, %6, -%0\n\tv_fmac_f32 %1, %4, %7\n\tv_fma_f32 %2, %8, %10, -%2\n\tv_fmac_f32 %3, %8, %11"
      : "=&v"(o.r), "=&v"(o.i), "=&v"(q.r), "=&v"(q.i)
      : "v"(a0.r), "v"(a0.i), "v"(b0.r), "v"(b0.i), "v"(a1.r), "v"(a1.i), "v"(b1.r), "v"(b1.i));
  r0 = o; r1 = q;
}
DEV float fadd1(float a, float b) { float o; asm("v_add_f32 %0, %1, %2" : "=v"(o) : "v"(a), "v"(b)); return o; }
DEV cf cseed(float a2, float br, float L) {
  const float e = __builtin_amdgcn_exp2f(a2 * L);
  const float ph = __builtin_amdgcn_fractf(br * L);
  return cf{e * __builtin_amdgcn_cosf(ph), e * __builtin_amdgcn_sinf(ph)};
}
template <int CTRL> DEV float dpp_add(float v) {
  return v + u2f(__builtin_amdgcn_update_dpp(0, f2u(v), CTRL, 0xf, 0xf, true));
}
struct Params {
  const char *x, *wqkv, *wo, *Are, *Aim, *Bre, *Bim, *Cre, *Cim, *logdt, *dskip, *gatew;
  char *ws, *out;
};

DEV u32x2 s_gen(const Params& P, int bid, int gw, int lane, u32x4 (&kv)[4] PROF_ARG) {
  char* ws = P.ws;
  const int d = 2 * bid + (gw >> 1), hw = gw & 1;
  const int pi = d * 128 + 64 * hw + lane;
  unsigned rdt, rds, rAre, rAim, rBre, rBim, rCre, rCim;
  u32x2 tag;
#define LDU16(dst, base, off) asm volatile("global_load_ushort %0, %1, %2" : "=v"(dst) : "v"(off), "s"(base) : "memory")
#define LD16(dst, base, off) asm volatile("global_load_dwordx4 %0, %1, %2" : "=v"(dst) : "v"(off), "s"(base) : "memory")
  asm volatile("global_load_dwordx2 %0, %1, %2 sc1" : "=v"(tag) : "v"(WS_EP), "s"(ws) : "memory");
  LDU16(rdt, P.logdt, d * 2); LDU16(rAre, P.Are, pi * 2); LDU16(rAim, P.Aim, pi * 2); LDU16(rBre, P.Bre, pi * 2);
  LDU16(rBim, P.Bim, pi * 2); LDU16(rCre, P.Cre, pi * 2); LDU16(rCim, P.Cim, pi * 2); LDU16(rds, P.dskip, d * 2);
  const int grow = 8 * bid + 2 * gw;
  u32x4 gx0, gx1, gwv;
  LD16(gx0, P.x, grow * 1024 + lane * 16); LD16(gx1, P.x, grow * 1024 + 1024 + lane * 16); LD16(gwv, P.gatew, lane * 16);
  PRIO(0);
  const unsigned reg = conv_region(gw >> 1) + hw * 17408, tab = B_TAB + (gw >> 1) * 5128;
  unsigned ea[16];
#pragma unroll
  for (int it = 0; it < 2; ++it)
#pragma unroll
    for (int e = 0; e < 8; e += 2) {
      const int l0 = 2047 - (1024 * hw + 512 * it + 8 * lane) - e;
      const unsigned pr = pack_bf16((float)l0, (float)(l0 - 1));
      ea[it * 8 + e] = tab + (l0 < 128 ? l0 : (int)(pr & 0xffffu) - 0x4280) * 8;
      ea[it * 8 + e + 1] = tab + (l0 - 1 < 128 ? l0 - 1 : (int)(pr >> 16) - 0x4280) * 8;
    }
#pragma unroll
  for (int i = 0; i < 16; ++i) asm volatile("" : "+v"(ea[i]));
  asm volatile("s_waitcnt vmcnt(3)" : "+v"(tag), "+v"(rdt), "+v"(rds), "+v"(rAre), "+v"(rAim), "+v"(rBre), "+v"(rBim), "+v"(rCre),
               "+v"(rCim)::"memory");
  if (gw == 0) {
    tag[0] += 1;
    *(volatile LDS u32x2*)(size_t)F_TAG = tag;
    lds_flag_set(F_TAGOK);
  }
  STAMP(1);
  const float dt = round_bf16(expf(bf_lo(rdt)));
  const float a2 = (dt * bf_lo(rAre)) * 1.4426950408889634f;
  const float br = (dt * bf_lo(rAim)) * 0.15915494309189535f;
  const cf w1 = cseed(a2, br, 1.f), W = cseed(a2, br, 128.f);
  cf w8c = cseed(a2, br, 8.f); w8c.i = -w8c.i;
  cf R = cmul(cf{bf_lo(rCre), -bf_lo(rCim)}, cf{dt * bf_lo(rBre), -(dt * bf_lo(rBim))});
  unsigned rv[16], lv[41];
#define PK(v) pack_bf16((v).r, (v).i)
  cf p[8], Wp[17];
  p[0] = cf{1.f, 0.f}; p[1] = w1; Wp[1] = W;
  lv[0] = 0x3f80u; lv[1] = PK(w1); lv[8] = PK(W); rv[0] = PK(R);
#pragma unroll
  for (int i = 2; i < 8; ++i) { cmul2(p[i], p[i - 1], w1, R, R, w8c); lv[i] = PK(p[i]); rv[i - 1] = PK(R); }
#pragma unroll
  for (int i = 1; i < 8; ++i) { cf t; cmul2(t, W, p[i], R, R, w8c); lv[8 + i] = PK(t); rv[6 + i] = PK(R); }
  cmul2(Wp[2], W, W, R, R, w8c); rv[14] = PK(R);
  cmul2(Wp[3], Wp[2], W, R, R, w8c); rv[15] = PK(R);
  lv[16] = PK(Wp[2]); lv[20] = PK(Wp[3]);
#pragma unroll
  for (int i = 1; i < 4; ++i) { cf t, u; cmul2(t, Wp[2], p[2 * i], u, Wp[3], p[2 * i]); lv[16 + i] = PK(t); lv[20 + i] = PK(u); }
  cmul2(Wp[4], Wp[2], Wp[2], Wp[5], Wp[3], Wp[2]);
  cmul2(Wp[6], Wp[3], Wp[3], Wp[7], Wp[4], Wp[3]);
#pragma unroll
  for (int a = 0; a < 4; a += 2) {
    cf t, u; cmul2(t, Wp[4 + a], p[4], u, Wp[5 + a], p[4]);
    lv[24 + 2 * a] = PK(Wp[4 + a]); lv[25 + 2 * a] = PK(t); lv[26 + 2 * a] = PK(Wp[5 + a]); lv[27 + 2 * a] = PK(u);
  }
#pragma unroll
  for (int a = 8; a < 16; a += 2) { cmul2(Wp[a], Wp[a - 4], Wp[4], Wp[a + 1], Wp[a - 3], Wp[4]); lv[24 + a] = PK(Wp[a]); lv[25 + a] = PK(Wp[a + 1]); }
  Wp[16] = cmul(Wp[8], Wp[8]); lv[40] = PK(Wp[16]);
#undef PK
#pragma unroll
  for (int i = 0; i < 16; ++i) asm volatile("" : "+v"(rv[i]));
#pragma unroll
  for (int i = 1; i < 41; ++i) asm volatile("" : "+v"(lv[i]));
  asm volatile("s_waitcnt vmcnt(0)" : "+v"(gx0), "+v"(gx1), "+v"(gwv)::"memory");
  {
    float s0 = 0.f, s1 = 0.f;
#pragma unroll
    for (int j = 0; j < 4; ++j) {
      const float wl = bf_lo(gwv[j]), wh = bf_hi(gwv[j]);
      s0 = __builtin_fmaf(bf_lo(gx0[j]), wl, s0); s0 = __builtin_fmaf(bf_hi(gx0[j]), wh, s0);
      s1 = __builtin_fmaf(bf_lo(gx1[j]), wl, s1); s1 = __builtin_fmaf(bf_hi(gx1[j]), wh, s1);
    }
    auto sw = __builtin_amdgcn_permlane32_swap(f2u(s0), f2u(s1), false, false);
    float s = u2f(sw[0]) + u2f(sw[1]);
    auto sx = __builtin_amdgcn_permlane16_swap(f2u(s), f2u(s), false, false);
    s = u2f(sx[0]) + u2f(sx[1]);
    s = dpp_add<0x128>(s); s = dpp_add<0x124>(s); s = dpp_add<0x122>(s); s = dpp_add<0x121>(s);
    const float gv = round_bf16(__builtin_amdgcn_rcpf(1.0f + __expf(-round_bf16(s))));
    if ((lane & 31) == 0) st4_dev(ws, WS_G + (grow + (lane >> 5)) * 4, f2u(gv));
  }
  STAMP(16);
  const int j = lane & 15, g = lane >> 4;
  unsigned t0a = tab + hw * 4 + (128 * (g >> 1) + 8 * j + 4 * (g & 1)) * 8;
  unsigned t1a = tab + hw * 4 + (g < 2 ? 256 + 64 * g + 4 * j : 384 + 64 * (g - 2) + 2 * j) * 8, t1b = t1a + (g < 2 ? 16 : 256);
  unsigned t2a = tab + hw * 4 + (512 + 64 * g + j) * 8;
  const float ds0 = (lane == 0 && hw == 0) ? bf_lo(rds) : 0.f;
  asm volatile("" : "+v"(t0a), "+v"(t1a), "+v"(t1b), "+v"(t2a));
  tag = get_tag();
  if (lane < 32) lds_w128(B_U + (gw >> 1) * 4608 + lane * 16, u32x4{0, 0, 0, 0});
  lds_wait<4, 1>(F_GD);
  PRIO(1);
  STAMP(3);
  const unsigned wa = reg + lane * 4, ra = reg + (lane & 15) * 272 + (lane >> 4) * 16;
#pragma unroll
  for (int i = 0; i < 16; ++i) lds_w32(wa + i * 272, rv[i]);
#pragma unroll
  for (int i = 0; i < 41; ++i) lds_w32(wa + 4352 + i * 272, lv[i]);
  CBAR();
  f32x4 d0 = {0, 0, 0, 0}, d1 = d0, d2 = d0;
#pragma unroll
  for (int ks = 0; ks < 4; ++ks) {
    const u32x4 bfr = lds_r128(ra + ks * 64);
    d0 = mfma16(lds_r128(ra + 4352 + ks * 64), bfr, d0);
    d1 = mfma16(lds_r128(ra + 8704 + ks * 64), bfr, d1);
    d2 = mfma16(lds_r128(ra + 13056 + ks * 64), bfr, d2);
  }
  CBAR();
  STAMP(17);
  d0[0] += ds0;
#define TW(a, v) *(LDS float*)(size_t)(a) = (v)
#pragma unroll
  for (int q = 0; q < 4; ++q) TW(t0a + 8 * q, d0[q]);
  TW(t1a, d1[0]); TW(t1a + 8, d1[1]); TW(t1b, d1[2]); TW(t1b + 8, d1[3]);
  if (lane <= 32) TW(t2a, d2[0]);
  if (g < 2) {
#pragma unroll
    for (int q = 1; q < 4; ++q) TW(t2a + 128 * q, d2[q]);
  }
#undef TW
  lds_flag_set(F_GP + gw * 4);
  STAMP(18);
  lds_wait<1, 0>(F_GP + (gw ^ 1) * 4);
  STAMP(19);
  f32x2 tp[19];
  float ta[19];
#pragma unroll
  for (int i = 0; i < 19; ++i) tp[i] = *(LDS f32x2*)(size_t)(i < 16 ? ea[i] : tab + (512 - (i - 16)) * 8);
  SB();
#pragma unroll
  for (int i = 0; i < 19; ++i) ta[i] = fadd1(tp[i][0], tp[i][1]);
#pragma unroll
  for (int it = 0; it < 2; ++it)
#pragma unroll
    for (int e = 0; e < 4; ++e) kv[it][e] = pack_bf16(ta[it * 8 + 2 * e], ta[it * 8 + 2 * e + 1]);
  {
    const float a = ta[16], b = ta[17], c = ta[18];
    const u32x4 nx = hw ? u32x4{0, 0, 0, 0} : u32x4{pack_bf16(a, a), pack_bf16(b, b), pack_bf16(b, c), pack_bf16(c, c)};
#pragma unroll
    for (int e = 0; e < 4; ++e) {
      kv[2][e] = __builtin_amdgcn_update_dpp(0u, lane == 0 ? kv[1][e] : kv[0][e], 0x134, 0xf, 0xf, false);
      kv[3][e] = __builtin_amdgcn_update_dpp(0u, lane == 0 ? nx[e] : kv[1][e], 0x134, 0xf, 0xf, false);
    }
  }
  asm volatile("" : "+v"(kv[0]), "+v"(kv[1]), "+v"(kv[2]), "+v"(kv[3])::"memory");
  STAMP(2);
  return tag;
}

DEV float max3(float a, float b, float c) { return __builtin_fmaxf(__builtin_fmaxf(a, b), c); }
DEV float rowmax16(f32x16 s) {
  return max3(max3(max3(s[0], s[1], s[2]), max3(s[3], s[4], s[5]), max3(s[6], s[7], s[8])),
              max3(max3(s[9], s[10], s[11]), max3(s[12], s[13], s[14]), s[15]), s[15]);
}
DEV float amax3(float a, float b, float c) { float o; asm("v_max3_f32 %0, %1, %2, %3" : "=v"(o) : "v"(a), "v"(b), "v"(c)); return o; }
DEV float halfmax(float m) {
  auto r = __builtin_amdgcn_permlane32_swap(f2u(m), f2u(m), false, false);
  return __builtin_fmaxf(u2f(r[0]), u2f(r[1]));
}
DEV f32x8 lo8(f32x16 v) { return __builtin_shufflevector(v, v, 0, 1, 2, 3, 4, 5, 6, 7); }
DEV f32x8 hi8(f32x16 v) { return __builtin_shufflevector(v, v, 8, 9, 10, 11, 12, 13, 14, 15); }
#define EXPS(S, a, b) _Pragma("unroll") for (int i_ = a; i_ < b; ++i_) { float e_ = S[i_]; asm volatile("v_exp_f32 %0, %0" : "+v"(e_)); S[i_] = e_; }
#define PV(O, V, P8) asm volatile("v_smfmac_f32_32x32x32_bf16 %0, %1, %2, %3" : "+v"(O) : "v"(V), "v"(P8), "v"(idx))
#define QK(D, Q, NM) asm volatile("v_mfma_f32_32x32x16_bf16 %0, %1, %2, %3" : "=v"(D) : "v"(kn), "v"(Q), "v"(NM))
#define TILE(T, VL, VH, XL, XH)                                    \
  EXPS(SA, 0, 2); SB();                                            \
  PV(OB, XH, hi8(SBv)); SB();                                      \
  EXPS(SA, 2, 6); SB();                                            \
  QK(SBv, qB, nm); SB();                                          \
  kn = lds_r128(kb + ((T) + 1) * KVS); SB();                       \
  EXPS(SA, 6, 11); SB();                                           \
  PV(OA, VL, lo8(SA)); SB();                                       \
  EXPS(SA, 11, 16); SB();                                          \
  EXPS(SBv, 0, 2); SB();                                           \
  PV(OA, VH, hi8(SA)); SB();                                       \
  XL = lds_r128(vb0 + ((T) + 1) * KVS); SB();                      \
  EXPS(SBv, 2, 6); SB();                                           \
  QK(SA, qA, nm); SB();                                           \
  XH = lds_r128(vb1 + ((T) + 1) * KVS); SB();                      \
  EXPS(SBv, 6, 11); SB();                                          \
  PV(OB, VL, lo8(SBv)); SB();                                      \
  EXPS(SBv, 11, 16); SB();

DEV void attn_exact(const char* kp, int t0, int t1, u32x4 q, f32x16& O, float& m, int lane) {
  const int r = lane & 31, idx = (int)0xDDDDDDDDu;
  const f32x16 zero16 = {0, 0, 0, 0, 0, 0, 0, 0, 0, 0, 0, 0, 0, 0, 0, 0};
  const unsigned one = (r == 16 || r == 20) ? 0x3f803f80u : 0u, vo = (WS_VF - WS_KF) + (lane >> 5) * 256 + (r & 15) * 16;
  float mm = -__builtin_inff();
  O = zero16;
#pragma clang loop unroll(disable)
  for (int pass = 0; pass < 2; ++pass) {
#pragma clang loop unroll(disable)
    for (int t = t0; t < t1; ++t) {
      u32x4 k, v0, v1;
      asm volatile("global_load_dwordx4 %0, %3, %5 sc1\n\tglobal_load_dwordx4 %1, %4, %5 sc1\n\tglobal_load_dwordx4 %2, %4, %5 offset:512 sc1\n\t"
                   "s_waitcnt vmcnt(0)" : "=&v"(k), "=&v"(v0), "=&v"(v1) : "v"(t * 1024 + lane * 16), "v"(t * 1024 + vo), "s"(kp) : "memory");
      f32x16 s = mfma32(k, q, zero16);
      if (pass == 0) { mm = __builtin_fmaxf(mm, rowmax16(s)); continue; }
      if (r >= 16) v0 = v1 = u32x4{one, one, one, one};
#pragma unroll
      for (int i = 0; i < 16; ++i) s[i] = __builtin_amdgcn_exp2f(s[i] - mm);
      O = __builtin_amdgcn_smfmac_f32_32x32x32_bf16(as_frag(v0), __builtin_bit_cast(bf16x16, lo8(s)), O, idx, 0, 0);
      O = __builtin_amdgcn_smfmac_f32_32x32x32_bf16(as_frag(v1), __builtin_bit_cast(bf16x16, hi8(s)), O, idx, 0, 0);
    }
    if (pass == 0) mm = halfmax(mm);
  }
  m = mm;
}

DEV void b_attn(char* ws, int bid, int w, int lane PROF_ARG) {
  const int head = 4 * (bid & 7) + ((bid >> 3) & 3), qt = (bid >> 5) * 8 + 2 * (w & 3), kh = w >> 2, t0 = kh * NKA;
  const int r = lane & 31, h = lane >> 5;
  const u32x2 tag = get_tag();
  unsigned kb = t0 * KVS + lane * 16;
  const unsigned oth = (r == 16 || r == 20) ? 2048 : 2064;
  unsigned vb0 = t0 * KVS + (r < 16 ? 1024 + h * 256 + r * 16 : oth);
  unsigned vb1 = t0 * KVS + (r < 16 ? 1536 + h * 256 + r * 16 : oth);
  int idx = (int)0xDDDDDDDDu;
  f32x16 OA, OB, SBv;
#pragma unroll
  for (int i = 0; i < 16; ++i) { OA[i] = 0.f; OB[i] = 0.f; SBv[i] = 0.f; }
  u32x4 xh = u32x4{0, 0, 0, 0};
  asm volatile("" : "+v"(SBv), "+v"(OA), "+v"(OB), "+v"(xh), "+v"(kb), "+v"(vb0), "+v"(vb1), "+v"(idx));
  STAMP(3);
  if (w == 0) {
    lds_wait<4, 2>(F_GD);
    const unsigned v = (lane & 1) ? 0u : 0x3f803f80u;
    lds_w128(((lane >> 1) + (lane < 32 ? 0 : NKA - 16)) * KVS + 2048 + (lane & 1) * 16, u32x4{v, v, v, v});
    lds_flag_set(F_ONES);
  }
  if (w == 4) {
    const int cq = (head >> 1) / 3, ck = (16 + (head >> 1)) / 3, cv = (32 + (head >> 1)) / 3;
    const int g = lane >> 4, i = lane & 15;
    poll1(ws, WS_FA + (g == 0 ? ck * 16 + i : g == 1 ? cv * 16 + i : cq * 16 + 2 * (bid >> 5) + (i & 1)) * 128, tag);
    lds_wait<1, 0>(F_ONES);
    lds_flag_set(F_GO);
  } else {
    if (w < 4) lds_wait<4, 4>(F_PD);
    lds_wait<1, 0>(F_GO);
  }
  STAMP(4);
  PRIO(2);
  u32x4 qA, qB, kn;
  const char* kp = ws + WS_KF + head * 65536;
  auto fetch = [&](int t) { dma16_dev(t * KVS, lane * 16, kp + t * 1024); dma16_dev(t * KVS + 1024, lane * 16, kp + (WS_VF - WS_KF) + t * 1024); };
  const int tw = t0 + (w & 3);
  asm volatile("global_load_dwordx4 %0, %3, %4 sc1\n\tglobal_load_dwordx4 %1, %3, %4 offset:1024 sc1\n\tglobal_load_dwordx4 %2, %3, %5 sc1"
               : "=&v"(qA), "=&v"(qB), "=&v"(kn) : "v"(lane * 16), "s"(ws + WS_QF + (head * 64 + qt) * 1024), "s"(kp + t0 * 1024) : "memory");
  fetch(tw); fetch(tw + 4);
  asm volatile("s_waitcnt vmcnt(4)" : "+v"(qA), "+v"(qB), "+v"(kn)::"memory");
  STAMP(10);
  const f32x16 zero16 = {0, 0, 0, 0, 0, 0, 0, 0, 0, 0, 0, 0, 0, 0, 0, 0};
  f32x16 SA = mfma32(kn, qA, zero16);
  f32x16 sb0 = mfma32(kn, qB, zero16);
  SB();
  WAIT_VM(2);
  lds_flag_set(F_F0 + w * 4);
  asm volatile("s_nop 11" : "+v"(SA), "+v"(sb0));
  float mA = amax3(amax3(amax3(SA[0], SA[1], SA[2]), amax3(SA[3], SA[4], SA[5]), amax3(SA[6], SA[7], SA[8])),
                   amax3(amax3(SA[9], SA[10], SA[11]), amax3(SA[12], SA[13], SA[14]), amax3(SA[15], sb0[0], sb0[1])),
                   amax3(amax3(sb0[2], sb0[3], sb0[4]), amax3(sb0[5], sb0[6], sb0[7]), amax3(sb0[8], sb0[9], sb0[10])));
  mA = halfmax(amax3(mA, amax3(sb0[11], sb0[12], sb0[13]), amax3(sb0[14], sb0[15], sb0[15])));
  float mB = mA;
  f32x16 nm;
#pragma unroll
  for (int i = 0; i < 16; ++i) { nm[i] = -mA; SA[i] = fadd1(SA[i], nm[0]); }
  asm volatile("" : "+v"(nm), "+v"(SA));
  STAMP(11);
  lds_wait<4, 0>(F_F0 + kh * 16);
  STAMP(5); RSTAMP(26);
  u32x4 vl = lds_r128(vb0), vh = lds_r128(vb1), xl;
#define PA0 4
#define PB0 5
  unsigned ev = 1u | (2u << 3) | (4u << 6) | (3u << 9) | (kh ? (PB0 < 6 ? 5u << (3 * PB0) : 0u) | (7u << 18) : (5u << (3 * PA0)) | (6u << 21));
#pragma clang loop unroll(disable)
  for (int g = kh ? (64 - NKA) / 4 : NKA / 4; g > 0; --g) {
    asm volatile("" : "+s"(ev));
    TILE(0, vl, vh, xl, xh)
    TILE(1, xl, xh, vl, vh)
    TILE(2, vl, vh, xl, xh)
    const unsigned e = ev & 7;
    if (e) {
      if (e == 1) { WAIT_VM(0); lds_flag_set(F_F2 + w * 4); fetch(tw + 8); fetch(tw + 12); lds_wait<4, 0>(F_F2 + kh * 16); }
      else if (e == 2) { WAIT_VM(0); lds_flag_set(F_F1 + w * 4); lds_wait<4, 0>(F_F1 + kh * 16); }
      else if (e == 3) lds_wait<4, 0>(F_FS);
      else if (e == 4) PRIO(1);
      else if (e == 5) PRIO(0);
      else if (e == 6) lds_flag_set(F_AD + 16 + w * 4);
      else lds_wait<4, 0>(F_AD + 16);
    }
    TILE(3, xl, xh, vl, vh)
    kb += 4 * KVS; vb0 += 4 * KVS; vb1 += 4 * KVS;
    ev >>= 3;
  }
  asm volatile("s_nop 1");
  PV(OB, xh, hi8(SBv));
  asm volatile("s_nop 15" : "+v"(OA), "+v"(OB));
  if (__builtin_expect(__any(!(OA[8] <= 0x1p30f && OA[8] >= 0x1p-60f)), 0)) attn_exact(kp, t0, kh ? 64 : NKA, qA, OA, mA, lane);
  if (__builtin_expect(__any(!(OB[8] <= 0x1p30f && OB[8] >= 0x1p-60f)), 0)) attn_exact(kp, t0, kh ? 64 : NKA, qB, OB, mB, lane);
  STAMP(6); RSTAMP(27);
  if (kh) PRIO(3); else lds_wait<4, 0>(F_AD + 16);
  {
    const f32x16 X = kh ? OA : OB;
    const unsigned ex = B_EX + w * 2560 + lane * 16;
    *(LDS f32x4*)(size_t)ex = f32x4{X[0], X[1], X[2], X[3]};
    *(LDS f32x4*)(size_t)(ex + 1024) = f32x4{X[4], X[5], X[6], X[7]};
    *(LDS f32x2*)(size_t)(B_EX + w * 2560 + 2048 + lane * 8) = f32x2{X[8], kh ? mA : mB};
  }
  lds_flag_set(F_EX + w * 4);
  {
    const f32x16 X = kh ? OB : OA;
    const float m = kh ? mB : mA;
    f32x4 y0, y1;
    f32x2 y2;
#pragma clang loop unroll(disable)
    for (int n = 0; n < (1 << 21); ++n) {
      unsigned f;
      asm volatile("ds_read_b32 %0, %4\n\tds_read_b128 %1, %5\n\tds_read_b128 %2, %5 offset:1024\n\tds_read_b64 %3, %6\n\ts_waitcnt lgkmcnt(0)"
                   : "=&v"(f), "=&v"(y0), "=&v"(y1), "=&v"(y2)
                   : "v"(F_EX + (w ^ 4) * 4), "v"(B_EX + (w ^ 4) * 2560 + lane * 16), "v"(B_EX + (w ^ 4) * 2560 + 2048 + lane * 8) : "memory");
      if (__builtin_amdgcn_readfirstlane(f)) break;
      __builtin_amdgcn_s_sleep(1);
    }
    PRIO(3);
    const float M = __builtin_fmaxf(m, y2[1]);
    float sx = __builtin_amdgcn_exp2f(m - M), sy = __builtin_amdgcn_exp2f(y2[1] - M);
    const float inv = __builtin_amdgcn_rcpf(__builtin_fmaf(X[8], sx, y2[0] * sy));
    sx *= inv; sy *= inv;
    unsigned ov[4];
#pragma unroll
    for (int j = 0; j < 2; ++j) {
      ov[j] = pack_bf16(__builtin_fmaf(X[2 * j], sx, y0[2 * j] * sy), __builtin_fmaf(X[2 * j + 1], sx, y0[2 * j + 1] * sy));
      ov[2 + j] = pack_bf16(__builtin_fmaf(X[4 + 2 * j], sx, y1[2 * j] * sy), __builtin_fmaf(X[5 + 2 * j], sx, y1[2 * j + 1] * sy));
    }
    st16_dev(ws, WS_QF + (head * 64 + qt + kh) * 1024 + lane * 16, u32x4{ov[0], ov[1], ov[2], ov[3]});
  }
  STAMP(12);
  publish(ws, WS_FO + ((qt + kh) * 32 + head) * 128, tag, lane);
  STAMP(7);
  const int T = (bid & 7) + 8 * ((bid >> 3) & 7);
  poll1_atomic(ws, WS_FO + (T * 32 + 4 * w + (lane & 3)) * 128, tag, lane < 4);
  STAMP(8);
#pragma unroll
  for (int i = 0; i < 4; ++i) dma16_dev(C_O + (4 * w + i) * 1024, lane * 16, ws + WS_QF + ((4 * w + i) * 64 + T) * 1024);
  WAIT_VM(0);
  lds_flag_set(F_OC + w * 4);
  STAMP(9); RSTAMP(28);
  if (bid == 0 && w == 0) {
    poll2(ws, WS_FY + lane * 128, WS_FY + (64 + lane) * 128, tag);
    poll2(ws, WS_FY + (128 + lane) * 128, WS_FY + (192 + lane) * 128, tag);
    if (lane == 0) st8_dev(ws, WS_EP, tag);
  }
}

#define MFMA16(D, A, B) asm volatile("v_mfma_f32_16x16x32_bf16 %0, %1, %2, %0" : "+v"(D) : "v"(A), "v"(B))
template <int CA, int CB>
DEV void conv_tiles(unsigned ka, char* ws, unsigned yo, unsigned ub) {
  f32x4 a0 = {0, 0, 0, 0}, a1 = a0, b0 = a0, b1 = a0;
  constexpr int NA = 8 * (CA + 2), NB = 8 * (CB + 2), LA = 4;
  u32x4 GA[8 + LA + 1], GB[8 + LA + 1], A[LA + 1];
  auto ga = [&](int q) { if (q < NA) GA[q % (8 + LA + 1)] = lds_r128(ub + 512 * (CA + 1) + 4096 - 64 * q); };
  auto gb = [&](int q) { if (q < NB) GB[q % (8 + LA + 1)] = lds_r128(ub + 512 * (CB + 1) + 4096 - 64 * q); };
  auto ra = [&](int q) { if (q < NB) A[q % (LA + 1)] = lds_r128(ka + 4032 - 64 * q); };
#pragma unroll
  for (int q = 0; q < LA; ++q) ra(q);
#pragma unroll
  for (int q = 0; q < 8 + LA; ++q) { gb(q); ga(q); }
  asm volatile("s_nop 1" : "+v"(a0), "+v"(a1), "+v"(b0), "+v"(b1));
#pragma unroll
  for (int s = 0; s < NB; ++s) {
    SB();
    MFMA16(b1, A[s % (LA + 1)], GB[s % (8 + LA + 1)]);
    SB();
    ra(s + LA);
    SB();
    if (s + 8 < NB) MFMA16(b0, A[s % (LA + 1)], GB[(s + 8) % (8 + LA + 1)]);
    SB();
    gb(s + 8 + LA);
    SB();
    if (s < NA) MFMA16(a1, A[s % (LA + 1)], GA[s % (8 + LA + 1)]);
    SB();
    ga(s + 8 + LA);
    SB();
    if (s + 8 < NA) MFMA16(a0, A[s % (LA + 1)], GA[(s + 8) % (8 + LA + 1)]);
  }
  asm volatile("s_nop 7" : "+v"(a0), "+v"(a1), "+v"(b0), "+v"(b1));
  st8_dev(ws, yo + 262144 * CA, u32x2{pack_bf16(a0[0], a0[1]), pack_bf16(a0[2], a0[3])});
  st8_dev(ws, yo + 262144 * (CA + 1), u32x2{pack_bf16(a1[0], a1[1]), pack_bf16(a1[2], a1[3])});
  st8_dev(ws, yo + 262144 * CB, u32x2{pack_bf16(b0[0], b0[1]), pack_bf16(b0[2], b0[3])});
  st8_dev(ws, yo + 262144 * (CB + 1), u32x2{pack_bf16(b1[0], b1[1]), pack_bf16(b1[2], b1[3])});
}

DEV void s_rest(const Params& P, u32x2 tag, int bid, int cw, int lane, const u32x4 (&kv)[4] PROF_ARG) {
  char* ws = P.ws;
  const int head = 4 * (bid & 7) + ((bid >> 3) & 3);
  const int d = 2 * bid + (cw >> 1), hw = cw & 1;
  {
#define CPRIO 1
    PRIO(CPRIO);
    const unsigned ur = B_U + (cw >> 1) * 4608;
    const unsigned tb = conv_region(cw >> 1), ta = tb + (128 * hw + lane) * 16;
    unsigned Od[2][7];
#pragma unroll
    for (int it = 0; it < 2; ++it)
#pragma unroll
      for (int k = 0; k < 7; ++k) {
        Od[it][k] = __builtin_amdgcn_alignbit(k < 3 ? kv[it][k + 1] : kv[2 + it][k - 3], k < 4 ? kv[it][k] : kv[2 + it][k - 4], 16);
        asm volatile("" : "+v"(Od[it][k]));
      }
    STAMP(20);
    if (hw == 1 && lane < 32) lds_w128(tb + (lane >> 2) * 4160 + 4096 + (lane & 3) * 16, u32x4{0, 0, 0, 0});
#pragma unroll
    for (int it = 0; it < 2; ++it) {
      const unsigned D[8] = {kv[it][0], kv[it][1], kv[it][2], kv[it][3], kv[2 + it][0], kv[2 + it][1], kv[2 + it][2], kv[2 + it][3]};
#pragma unroll
      for (int e = 0; e < 8; ++e) {
        const int k = e >> 1;
        lds_w128(ta + it * 1024 + e * 4160,
                 (e & 1) ? u32x4{Od[it][k], Od[it][k + 1], Od[it][k + 2], Od[it][k + 3]} : u32x4{D[k], D[k + 1], D[k + 2], D[k + 3]});
      }
    }
    STAMP(21);
    lds_flag_set(F_T1 + cw * 4);
    lds_wait<1, 0>(F_XGO);
    lds_wait<1, 0>(F_T1 + (cw ^ 1) * 4);
    STAMP(4);
    const int j = lane & 15, g = lane >> 4;
    unsigned ka = tb + ((7 - j) & 7) * 4160 + (2024 + 8 * g - 8 * (j >> 3)) * 2 - 4032;
    unsigned ub = ur + 512 - 32 + 32 * j + 16 * g - 4096;
    asm volatile("" : "+v"(ka), "+v"(ub));
    const unsigned yo = WS_XT + ((j >> 1) * 512 + d) * 64 + (16 * (j & 1) + 4 * g) * 2;
    if (hw) conv_tiles<2, 4>(ka, ws, yo, ub);
    else conv_tiles<0, 6>(ka, ws, yo, ub);
    lds_flag_set(F_CD + cw * 4);
    STAMP(5);
    lds_wait<4, 0>(F_CD);
    lds_wait<8, 1>(F_F0);
    const char* kp = ws + WS_KF + head * 65536;
#pragma unroll
    for (int i = 0; i < 8; ++i) {
      const int n = 4 * i + cw, t = n < NKA - 16 ? 16 + n : 32 + n;
      dma16_dev(t * KVS, lane * 16, kp + t * 1024);
      dma16_dev(t * KVS + 1024, lane * 16, kp + (WS_VF - WS_KF) + t * 1024);
    }
    if (lane < 16) {
      const int n = 4 * (lane >> 1) + cw, t = n < NKA - 16 ? 16 + n : 32 + n;
      const unsigned v = (lane & 1) ? 0u : 0x3f803f80u;
      lds_w128(t * KVS + 2048 + (lane & 1) * 16, u32x4{v, v, v, v});
    }
    WAIT_VM(0);
    lds_flag_set(F_FS + cw * 4);
    STAMP(6);
    PRIO(3);
    publish(ws, WS_FY + bid * 128 + cw * 8, tag, lane);
  }
  const int T = (bid & 7) + 8 * ((bid >> 3) & 7), dt = 4 * (bid >> 6) + cw;
  const int g = lane >> 4, q4 = (lane & 15) >> 2, pp = lane & 3;
  const unsigned st = C_ST + cw * 7424;
  const unsigned wa = st + (4 * (g >> 1) + q4) * 64 + (g & 1) * 32 + pp * 8;
  unsigned vw[4];
#pragma unroll
  for (int i = 0; i < 4; ++i) vw[i] = (lane >> 2) * 1024 + (lane & 3) * 16 + i * (16384 - 1024);
  u32x4 wf[28];
#pragma unroll
  for (int m = 0; m < 8; ++m) {
    dma16x4(st, vw, P.wo + dt * 64 + m * 65536);
    if (m == 7) break;
    WAIT_VM(0);
#pragma unroll
    for (int i = 0; i < 4; ++i) {
      const u32x2 lo = lds_tr(wa + i * 1024), hi = lds_tr(wa + i * 1024 + 512);
      wf[4 * m + i] = u32x4{lo[0], lo[1], hi[0], hi[1]};
    }
    asm volatile("s_waitcnt lgkmcnt(0)" : "+v"(wf[4 * m]), "+v"(wf[4 * m + 1]), "+v"(wf[4 * m + 2]), "+v"(wf[4 * m + 3])::"memory");
  }
  STAMP(7);
  const int tl = lane & 31, h = lane >> 5, t = 32 * T + tl;
  poll2(ws, WS_FY + (16 * dt + (lane >> 2)) * 128 + (lane & 3) * 8, WS_FY + (4 * T + ((lane >> 2) & 3)) * 128 + (lane & 3) * 8, tag);
  dma16_dev(st + 4096, lane * 16, ws + WS_XT + (T * 512 + 32 * dt) * 64);
  dma16_dev(st + 5120, lane * 16, ws + WS_XT + (T * 512 + 32 * dt) * 64 + 1024);
  float gt;
  asm volatile("global_load_dword %0, %1, %2 sc1\n\ts_waitcnt vmcnt(0)" : "=&v"(gt) : "v"(t * 4), "s"(ws + WS_G) : "memory");
  f32x16 acc;
#pragma unroll
  for (int i = 0; i < 16; ++i) acc[i] = 0.f;
  asm volatile("" : "+v"(acc));
  STAMP(8);
  const unsigned oa = C_O + lane * 16;
  PRIO(0); lds_wait<1, 8>(F_EX + 28); PRIO(3);
  unsigned have = 0;
  const unsigned oca = F_OC + (lane & 7) * 4;
  auto need = [&](int m) {
#pragma clang loop unroll(disable)
    for (int n = 0; !(have & (1u << m)) && n < (1 << 21); ++n) {
      unsigned f;
      asm volatile("ds_read_b32 %0, %1\n\ts_waitcnt lgkmcnt(0)" : "=&v"(f) : "v"(oca) : "memory");
      have = (unsigned)__ballot(f != 0);
    }
    CBAR();
  };
  need(0);
  STAMP(11);
  u32x4 of[3], wl[2];
  u32x2 y[4];
  float yb[16];
  const float omg = 1.0f - gt;
  of[0] = lds_r128(oa); of[1] = lds_r128(oa + 1024);
#pragma unroll
  for (int m = 0; m < 4; ++m) y[m] = lds_tr(st + 4096 + (8 * m + 4 * (g >> 1) + q4) * 64 + (g & 1) * 32 + pp * 8);
#pragma unroll
  for (int k = 0; k < 32; ++k) {
    SB();
    if (k < 28) MFMA32(acc, wf[k], of[k % 3]); else MFMA32(acc, wl[k & 1], of[k % 3]);
    SB();
    if ((k & 3) == 2 && k < 30) need((k >> 2) + 1);
    if (k < 30) of[(k + 2) % 3] = lds_r128(oa + (k + 2) * 1024);
    if (k >= 26 && k < 30) {
      const u32x2 lo = lds_tr(wa + (k - 26) * 1024), hi = lds_tr(wa + (k - 26) * 1024 + 512);
      wl[k & 1] = u32x4{lo[0], lo[1], hi[0], hi[1]};
    }
    if (k >= 8 && k < 16) {
      yb[2 * (k - 8)] = omg * bf_lo(y[(k - 8) >> 1][k & 1]); yb[2 * (k - 8) + 1] = omg * bf_hi(y[(k - 8) >> 1][k & 1]);
      asm volatile("" : "+v"(yb[2 * (k - 8)]), "+v"(yb[2 * (k - 8) + 1]));
    }
  }
  asm volatile("s_nop 15" : "+v"(acc));
  STAMP(10);
#pragma unroll
  for (int m = 0; m < 4; m += 2) {
    u32x2 a, b;
#pragma unroll
    for (int e = 0; e < 2; ++e) {
      a[e] = pack_bf16(__builtin_fmaf(gt, acc[4 * m + 2 * e], yb[4 * m + 2 * e]), __builtin_fmaf(gt, acc[4 * m + 2 * e + 1], yb[4 * m + 2 * e + 1]));
      b[e] = pack_bf16(__builtin_fmaf(gt, acc[4 * m + 4 + 2 * e], yb[4 * m + 4 + 2 * e]), __builtin_fmaf(gt, acc[4 * m + 5 + 2 * e], yb[4 * m + 5 + 2 * e]));
    }
    auto s0 = __builtin_amdgcn_permlane32_swap(a[0], b[0], false, false);
    auto s1 = __builtin_amdgcn_permlane32_swap(a[1], b[1], false, false);
    const unsigned r0 = s0[0], r1 = s1[0], r2 = s0[1], r3 = s1[1];
    *(u32x4*)(P.out + (size_t)(t * 1024 + (32 * dt + 8 * m + 8 * h) * 2)) = u32x4{r0, r1, r2, r3};
  }
}

__global__ __launch_bounds__(768, 3) void gated_mix_kernel(Params P) {
  const int bid = blockIdx.x, lane = threadIdx.x & 63;
  const int w = __builtin_amdgcn_readfirstlane(threadIdx.x >> 6);
  PROF_DECL;
  if (threadIdx.x < 32) asm volatile("ds_write_b128 %0, %1\n\ts_waitcnt lgkmcnt(0)" ::"v"(FL + lane * 16), "v"(u32x4{0, 0, 0, 0}) : "memory");
  __builtin_amdgcn_s_barrier();
  if (__builtin_expect(w < 8, 1)) {
    const int xcd = bid & 7, j = bid >> 3;
    const int tb = 4 * (xcd >> 1) + (j & 3), c = 8 * (xcd & 1) + (j >> 2);
    const int s0 = (2 * (xcd >> 1) + 4 * (xcd & 1)) & 7;
    if (__builtin_expect(w < 4, 1)) {
      a_load(P.x, P.wqkv, P.ws, bid, tb, c, s0, w, lane PROF_PASS);
    } else {
      PRIO(3);
      if (c < 10) a_gemm<0>(P.ws, tb, c, w - 4, lane PROF_PASS);
      else if (c == 10) a_gemm<1>(P.ws, tb, c, w - 4, lane PROF_PASS);
      else a_gemm<3>(P.ws, tb, c, w - 4, lane PROF_PASS);
    }
    b_attn(P.ws, bid, w, lane PROF_PASS);
  } else {
    PRIO(3);
    u32x4 kv[4];
    const u32x2 tag = s_gen(P, bid, w - 8, lane, kv PROF_PASS);
    s_rest(P, tag, bid, w - 8, lane, kv PROF_PASS);
  }
  PROF_END(0, P.ws);
}

extern "C" void kernel_launch(void* const* d_in, const int* in_sizes, int n_in, void* d_out, int out_size, void* d_ws,
                              size_t ws_size, hipStream_t stream) {
  (void)in_sizes; (void)n_in; (void)out_size; (void)ws_size;
  (void)hipFuncSetAttribute((const void*)gated_mix_kernel, hipFuncAttributeMaxDynamicSharedMemorySize, LDS_BYTES);
  Params P;
  P.x = (const char*)d_in[0]; P.wqkv = (const char*)d_in[1]; P.wo = (const char*)d_in[2];
  P.Are = (const char*)d_in[4]; P.Aim = (const char*)d_in[5]; P.Bre = (const char*)d_in[6]; P.Bim = (const char*)d_in[7];
  P.Cre = (const char*)d_in[8]; P.Cim = (const char*)d_in[9]; P.logdt = (const char*)d_in[10]; P.dskip = (const char*)d_in[11];
  P.gatew = (const char*)d_in[12]; P.ws = (char*)d_ws; P.out = (char*)d_out;
  gated_mix_kernel<<<256, 768, LDS_BYTES, stream>>>(P);
}
```

```cpp
#include <hip/hip_runtime.h>
#include <hip/hip_bf16.h>
#include <stdint.h>

typedef __attribute__((ext_vector_type(16))) float f32x16;
typedef __attribute__((ext_vector_type(8))) float f32x8;
typedef __attribute__((ext_vector_type(4))) float f32x4;
typedef __attribute__((ext_vector_type(2))) float f32x2;
typedef __attribute__((ext_vector_type(2))) __bf16 bf16x2;
typedef __attribute__((ext_vector_type(8))) __bf16 bf16x8;
typedef __attribute__((ext_vector_type(16))) __bf16 bf16x16;
typedef __attribute__((ext_vector_type(4))) short s16x4;
typedef __attribute__((ext_vector_type(4))) unsigned u32x4;
typedef __attribute__((ext_vector_type(2))) unsigned u32x2;
#define LDS __attribute__((address_space(3)))
#define DEV __device__ __forceinline__

constexpr unsigned WS_QF = 0;
constexpr unsigned WS_KF = 2u << 20;
constexpr unsigned WS_VF = 4u << 20;
constexpr unsigned WS_XT = 6u << 20;
constexpr unsigned WS_G  = 8u << 20;
constexpr unsigned WS_EP = WS_G + 8192;
constexpr unsigned WS_FA = WS_G + 16384;
constexpr unsigned WS_FX = WS_FA + 32768;
constexpr unsigned WS_FY = WS_FX + 32768;
constexpr unsigned WS_FO = WS_FY + 32768;

constexpr unsigned A_W = 0;
constexpr unsigned A_X = 61440;
constexpr unsigned B_U = 143360;
constexpr unsigned B_TAB = 152576;
DEV unsigned conv_region(int p) { return p ? 108160u : 33280u; }
constexpr unsigned KVS = 2080;
constexpr int NKA = 36;
constexpr unsigned B_EX = 0;
constexpr unsigned C_O = 20480;
constexpr unsigned C_ST = 133376;
constexpr unsigned FL = 163328;
constexpr unsigned F_LANDED = FL,
                   F_EX = FL + 32,
                   F_DONE = FL + 64,
                   F_ONES = FL + 112, F_XD = FL + 128, F_XGO = FL + 132, F_GO = FL + 136, F_TAGOK = FL + 140,
                   F_GP = FL + 144,
                   F_GD = FL + 176,
                   F_F0 = FL + 192, F_F1 = FL + 224, F_F2 = FL + 416, F_FS = FL + 256,
                   F_CD = FL + 272,
                   F_AD = FL + 288,
                   F_OC = FL + 320,
                   F_TAG = FL + 352, F_PD = FL + 368, F_T1 = FL + 384;
constexpr unsigned LDS_BYTES = 163840;

DEV unsigned pack_bf16(float lo, float hi) {
  f32x2 a = {lo, hi};
  return __builtin_bit_cast(unsigned, __builtin_convertvector(a, bf16x2));
}
DEV float u2f(unsigned u) { return __builtin_bit_cast(float, u); }
DEV unsigned f2u(float f) { return __builtin_bit_cast(unsigned, f); }
DEV float bf_lo(unsigned u) { return u2f(u << 16); }
DEV float bf_hi(unsigned u) { return u2f(u & 0xffff0000u); }
DEV float round_bf16(float f) { return bf_lo(pack_bf16(f, f)); }

#define WAIT_VM(n) asm volatile("s_waitcnt vmcnt(" #n ")" ::: "memory")
#define CBAR() asm volatile("" ::: "memory")
#define SB() __builtin_amdgcn_sched_barrier(0)
#define PRIO(n) __builtin_amdgcn_s_setprio(n)
DEV void dma16_dev(unsigned lds_addr, unsigned voff, const void* sbase) {
  asm volatile("s_mov_b32 m0, %0\n\ts_nop 0\n\tglobal_load_lds_dwordx4 %1, %2 sc1" ::"s"(lds_addr), "v"(voff), "s"(sbase) : "memory");
}
DEV void dma16x4(unsigned lds_addr, const unsigned* v, const void* sbase) {
  asm volatile("s_mov_b32 m0, %0\n\ts_nop 0\n\tglobal_load_lds_dwordx4 %1, %5\n\tglobal_load_lds_dwordx4 %2, %5 offset:1024\n\t"
               "global_load_lds_dwordx4 %3, %5 offset:2048\n\tglobal_load_lds_dwordx4 %4, %5 offset:3072"
               ::"s"(lds_addr), "v"(v[0]), "v"(v[1]), "v"(v[2]), "v"(v[3]), "s"(sbase) : "memory");
}
DEV u32x4 lds_r128(unsigned a) { return *(LDS u32x4*)(size_t)a; }
DEV void lds_w128(unsigned a, u32x4 v) { *(LDS u32x4*)(size_t)a = v; }
DEV void lds_w32(unsigned a, unsigned v) { *(LDS unsigned*)(size_t)a = v; }
DEV u32x2 lds_tr(unsigned a) { return __builtin_bit_cast(u32x2, __builtin_amdgcn_ds_read_tr16_b64_v4i16((LDS s16x4*)(size_t)a)); }
DEV bf16x8 as_frag(u32x4 v) { return __builtin_bit_cast(bf16x8, v); }
DEV f32x16 mfma32(u32x4 a, u32x4 b, f32x16 c) { return __builtin_amdgcn_mfma_f32_32x32x16_bf16(as_frag(a), as_frag(b), c, 0, 0, 0); }
DEV f32x4 mfma16(u32x4 a, u32x4 b, f32x4 c) { return __builtin_amdgcn_mfma_f32_16x16x32_bf16(as_frag(a), as_frag(b), c, 0, 0, 0); }
#define MFMA32(D, A, B) asm volatile("v_mfma_f32_32x32x16_bf16 %0, %1, %2, %0" : "+v"(D) : "v"(A), "v"(B))
DEV void st16_dev(const void* base, unsigned off, u32x4 v) { asm volatile("global_store_dwordx4 %0, %1, %2 sc1\n\ts_nop 1" ::"v"(off), "v"(v), "s"(base) : "memory"); }
DEV void st8_dev(const void* base, unsigned off, u32x2 v) { asm volatile("global_store_dwordx2 %0, %1, %2 sc1" ::"v"(off), "v"(v), "s"(base) : "memory"); }
DEV void st4_dev(const void* base, unsigned off, unsigned v) { asm volatile("global_store_dword %0, %1, %2 sc1" ::"v"(off), "v"(v), "s"(base) : "memory"); }

DEV void lds_flag_set(unsigned a) { asm volatile("ds_write_b32 %0, %1" ::"v"(a), "v"(1u) : "memory"); }
template <int NW, int SLEEP> DEV void lds_wait(unsigned a) {
#pragma clang loop unroll(disable)
  for (int n = 0; n < (1 << 21); ++n) {
    unsigned ok;
    if (NW == 8) {
      u32x4 v, u;
      asm volatile("ds_read_b128 %0, %2\n\tds_read_b128 %1, %2 offset:16\n\ts_waitcnt lgkmcnt(0)" : "=&v"(v), "=&v"(u) : "v"(a) : "memory");
      ok = v[0] & v[1] & v[2] & v[3] & u[0] & u[1] & u[2] & u[3];
    } else if (NW == 4) {
      u32x4 v;
      asm volatile("ds_read_b128 %0, %1\n\ts_waitcnt lgkmcnt(0)" : "=&v"(v) : "v"(a) : "memory");
      ok = v[0] & v[1] & v[2] & v[3];
    } else {
      asm volatile("ds_read_b32 %0, %1\n\ts_waitcnt lgkmcnt(0)" : "=&v"(ok) : "v"(a) : "memory");
    }
    if (__builtin_amdgcn_readfirstlane(ok)) break;
    if (SLEEP) __builtin_amdgcn_s_sleep(SLEEP);
  }
}
DEV u32x2 get_tag() {
  lds_wait<1, 1>(F_TAGOK);
  return *(volatile LDS u32x2*)(size_t)F_TAG;
}
constexpr int PSLEEP = 4;
DEV void publish(char* ws, unsigned off, u32x2 tag, int lane) {
  WAIT_VM(0);
  if (lane == 0) st8_dev(ws, off, tag);
}
DEV void poll1(const char* ws, unsigned off, u32x2 tag) {
#pragma clang loop unroll(disable)
  for (int n = 0; n < (1 << 17); ++n) {
    u32x2 a;
    asm volatile("global_load_dwordx2 %0, %1, %2 sc1\n\ts_waitcnt vmcnt(0)" : "=&v"(a) : "v"(off), "s"(ws) : "memory");
    if (!__any(a[0] != tag[0] || a[1] != tag[1])) break;
    __builtin_amdgcn_s_sleep(PSLEEP);
  }
}
DEV void poll1_atomic(const char* ws, unsigned off, u32x2 tag, bool on) {
#pragma clang loop unroll(disable)
  for (int n = 0; n < (1 << 17); ++n) {
    u32x2 a = tag;
    if (on) asm volatile("global_atomic_add_x2 %0, %1, %2, %3 sc0 sc1\n\ts_waitcnt vmcnt(0)" : "=&v"(a) : "v"(off), "v"(u32x2{0, 0}), "s"(ws) : "memory");
    on = a[0] != tag[0] || a[1] != tag[1];
    if (!__any(on)) break;
    __builtin_amdgcn_s_sleep(PSLEEP);
  }
}
DEV void poll2(const char* ws, unsigned off0, unsigned off1, u32x2 tag) {
#pragma clang loop unroll(disable)
  for (int n = 0; n < (1 << 17); ++n) {
    u32x2 a, b;
    asm volatile("global_load_dwordx2 %0, %2, %4 sc1\n\tglobal_load_dwordx2 %1, %3, %4 sc1\n\ts_waitcnt vmcnt(0)"
                 : "=&v"(a), "=&v"(b) : "v"(off0), "v"(off1), "s"(ws) : "memory");
    if (!__any(a[0] != tag[0] || a[1] != tag[1] || b[0] != tag[0] || b[1] != tag[1])) break;
    __builtin_amdgcn_s_sleep(PSLEEP);
  }
}

constexpr int STAG = 6;
DEV void a_load(const char* x, const char* wq, char* ws, int bid, int tb, int c, int s0, int l, int lane) {
  PRIO(2);
  unsigned vw[12], vx[16];
#pragma unroll
  for (int n = 0; n < 12; ++n) {
    const int m = 64 * (n % 3) + lane, row = m / 12;
    vw[n] = row * 3072 + (m - 12 * row) * 16 + (n / 3) * 49152 + 3072 - (n & 3) * 1024;
  }
#pragma unroll
  for (int n = 0; n < 16; ++n)
    vx[n] = (lane >> 3) * 1024 + (((lane & 7) ^ (4 * (n & 1) + (lane >> 4))) << 4) + (n >> 2) * 32768 + (n & 3) * 7168;
  const int rot = (tb + c) & 3;
  wq += 192 * c - 3072;
  x += (size_t)(128 * tb) * 1024;
  for (int n = 0; n < l; ++n) __builtin_amdgcn_s_sleep(STAG);
  auto issue = [&](int i) {
    const int s = (s0 + (i & 4) + ((i + rot) & 3)) & 7, sl = i % 5;
#pragma unroll
    for (int q = 0; q < 4; ++q) {
      dma16x4(A_X + q * 20480 + sl * 4096, vx + 4 * q, x + s * 128);
      if (q < 3) dma16x4(A_W + sl * 12288 + q * 4096, vw + 4 * q, wq + (size_t)s * (64 * 3072));
    }
  };
  issue(l);
  WAIT_VM(0);
  lds_flag_set(F_LANDED + l * 4);
  const int xh = (c >> 2) & 1, xb = 2 * ((s0 + rot) & 7) + xh;
  if (l == 0) {
    const int g = lane >> 4, q4 = (lane & 15) >> 2, pp = lane & 3;
    u32x2 t[4][4];
#pragma unroll
    for (int m = 0; m < 4; ++m) {
      const int row = 8 * g + 4 * (m & 1) + q4;
      const int ch = (4 * xh + 2 * (m >> 1) + (pp >> 1)) ^ ((row >> 1) & 7);
#pragma unroll
      for (int q = 0; q < 4; ++q) t[q][m] = lds_tr(A_X + row * 128 + ch * 16 + (pp & 1) * 8 + q * 20480);
    }
    lds_flag_set(F_XD);
    issue(4);
    const unsigned off = WS_XT + (4 * tb * 512 + 32 * xb + (lane & 15)) * 64 + g * 16;
#pragma unroll
    for (int q = 0; q < 4; ++q) {
      st16_dev(ws, off + q * 32768, u32x4{t[q][0][0], t[q][0][1], t[q][1][0], t[q][1][1]});
      st16_dev(ws, off + q * 32768 + 1024, u32x4{t[q][2][0], t[q][2][1], t[q][3][0], t[q][3][1]});
    }
  }
  if (l > 0) {
    lds_wait<4, 0>(F_DONE + (l - 1) * 16);
    if (l == 1) lds_wait<1, 0>(F_XD);
    issue(l + 4);
  }
  if (l == 0) WAIT_VM(8); else WAIT_VM(0);
  lds_flag_set(F_LANDED + (l + 4) * 4);
  if (l == 0) publish(ws, WS_FX + (xb * 16 + tb) * 128, get_tag(), lane);
  PRIO(0);
  if (l == 1) {
    poll1(ws, WS_FX + ((bid >> 4) * 16 + (lane & 15)) * 128, get_tag());
#pragma unroll
    for (int i = 0; i < 8; ++i)
      dma16_dev(B_U + (i >> 2) * 4608 + 512 + (i & 3) * 1024, (lane >> 2) * 32768 + (lane & 3) * 16,
                ws + WS_XT + (2 * bid + (i >> 2)) * 64 + (i & 3) * (16 * 32768));
    WAIT_VM(0);
    lds_flag_set(F_XGO);
  }
}

template <int NV>
DEV void a_gemm(char* ws, int tb, int c, int w, int lane) {
  const int r = lane & 31, h = lane >> 5;
  const unsigned xr = A_X + w * 20480;
  unsigned xa[4];
#pragma unroll
  for (int ks = 0; ks < 4; ++ks) { xa[ks] = xr + r * 128 + ((((h ^ (r >> 1)) & 7) << 4) ^ (ks << 5)); asm volatile("" : "+v"(xa[ks])); }
  const int g = lane >> 4, q4 = (lane & 15) >> 2, pp = lane & 3;
  unsigned wa = A_W + (8 * (g >> 1) + q4) * 192 + (g & 1) * 32 + pp * 8;
  asm volatile("" : "+v"(wa));
  const int T = 4 * tb + w;
  f32x16 acc[3];
#pragma unroll
  for (int p = 0; p < 3; ++p)
#pragma unroll
    for (int i = 0; i < 16; ++i) acc[p][i] = 0.f;
  asm volatile("" : "+v"(acc[0]), "+v"(acc[1]), "+v"(acc[2]));
  const u32x2 tag = get_tag();
  lds_wait<1, 0>(F_LANDED);
  constexpr int PF = 2, NB = PF + 1;
  u32x4 xf[NB];
  u32x2 wl[NB][3], wh[NB][3];
  auto rdx = [&](int k) { xf[k % NB] = lds_r128(xa[k & 3] + ((k >> 2) % 5) * 4096); };
  auto rdw = [&](int k, int p) {
    wl[k % NB][p] = lds_tr(wa + ((k >> 2) % 5) * 12288 + (k & 3) * 3072 + p * 64);
    wh[k % NB][p] = lds_tr(wa + ((k >> 2) % 5) * 12288 + (k & 3) * 3072 + p * 64 + 768);
  };
#pragma unroll
  for (int k = 0; k < PF; ++k) { rdx(k); rdw(k, 0); rdw(k, 1); rdw(k, 2); }
  unsigned pk = 0;
#pragma unroll
  for (int k = 0; k < 32; ++k) {
    const int i = k >> 2, ks = k & 3;
    SB();
#pragma unroll
    for (int p = 0; p < 3; ++p) {
      const u32x4 wf = {wl[k % NB][p][0], wl[k % NB][p][1], wh[k % NB][p][0], wh[k % NB][p][1]};
      if (p >= 3 - NV) MFMA32(acc[p], xf[k % NB], wf); else MFMA32(acc[p], wf, xf[k % NB]);
      SB();
      if (ks == 4 - PF && p == 0 && i < 7) {
        if (__builtin_expect(!__builtin_amdgcn_readfirstlane(pk), 0)) lds_wait<1, 0>(F_LANDED + (i + 1) * 4);
        CBAR();
      }
      if (k + PF < 32) { if (p == 0) rdx(k + PF); rdw(k + PF, p); }
      if (ks == 3 - PF && p == 1 && i < 7) pk = *(volatile LDS unsigned*)(size_t)(F_LANDED + (i + 1) * 4);
      if (ks == 3 - PF && p == 2) {
        if (i < 3) lds_flag_set(F_DONE + i * 16 + w * 4);
        else if (i == 7) lds_flag_set(F_GD + w * 4);
      }
      SB();
    }
  }
  asm volatile("s_nop 15" : "+v"(acc[0]), "+v"(acc[1]), "+v"(acc[2]));
#pragma unroll
  for (int p = 0; p < 3; ++p) {
    const int G = 3 * c + p, hp = G & 15;
    if (p >= 3 - NV) {
      const unsigned off = WS_VF + ((2 * hp + (r >> 4)) * 64 + T) * 1024 + h * 256 + (r & 15) * 16;
#pragma unroll
      for (int s = 0; s < 2; ++s) {
        u32x4 v;
#pragma unroll
        for (int j = 0; j < 4; ++j) v[j] = pack_bf16(acc[p][8 * s + 2 * j], acc[p][8 * s + 2 * j + 1]);
        st16_dev(ws, off + s * 512, v);
      }
    } else {
      const bool isq = G < 16;
      const float sc = isq ? 0.36067376022224085f : 1.0f;
      const int slotr = isq ? r : ((r & 19) | ((r & 4) << 1) | ((r & 8) >> 1));
      const unsigned off = (isq ? WS_QF : WS_KF) + (2 * hp * 64 + T) * 1024 + h * 512 + slotr * 16;
#pragma unroll
      for (int s = 0; s < 2; ++s) {
        u32x4 v;
#pragma unroll
        for (int j = 0; j < 4; ++j) v[j] = pack_bf16(acc[p][8 * s + 2 * j] * sc, acc[p][8 * s + 2 * j + 1] * sc);
        st16_dev(ws, off + s * 65536, v);
      }
    }
  }
  WAIT_VM(0);
  lds_flag_set(F_PD + w * 4);
  if (w == 0) {
    lds_wait<4, 0>(F_PD);
    if (lane == 0) st8_dev(ws, WS_FA + (c * 16 + tb) * 128, tag);
  }
}

struct cf { float r, i; };
DEV cf cmul(cf a, cf b) {
  cf o;
  asm("v_mul_f32 %0, %3, %5\n\tv_mul_f32 %1, %3, %4\n\tv_fma_f32 %0, %2, %4, -%0\n\tv_fmac_f32 %1, %2, %5"
      : "=&v"(o.r), "=&v"(o.i) : "v"(a.r), "v"(a.i), "v"(b.r), "v"(b.i));
  return o;
}
DEV void cmul2(cf& r0, cf a0, cf b0, cf& r1, cf a1, cf b1) {
  cf o, q;
  asm("v_mul_f32 %0, %5, %7\n\tv_mul_f32 %1, %5, %6\n\tv_mul_f32 %2, %9, %11\n\tv_mul_f32 %3, %9, %10\n\t"
      "v_fma_f32 %0, %4, %6, -%0\n\tv_fmac_f32 %1, %4, %7\n\tv_fma_f32 %2, %8, %10, -%2\n\tv_fmac_f32 %3, %8, %11"
      : "=&v"(o.r), "=&v"(o.i), "=&v"(q.r), "=&v"(q.i)
      : "v"(a0.r), "v"(a0.i), "v"(b0.r), "v"(b0.i), "v"(a1.r), "v"(a1.i), "v"(b1.r), "v"(b1.i));
  r0 = o; r1 = q;
}
DEV float fadd1(float a, float b) { float o; asm("v_add_f32 %0, %1, %2" : "=v"(o) : "v"(a), "v"(b)); return o; }
DEV cf cseed(float a2, float br, float L) {
  const float e = __builtin_amdgcn_exp2f(a2 * L);
  const float ph = __builtin_amdgcn_fractf(br * L);
  return cf{e * __builtin_amdgcn_cosf(ph), e * __builtin_amdgcn_sinf(ph)};
}
template <int CTRL> DEV float dpp_add(float v) {
  return v + u2f(__builtin_amdgcn_update_dpp(0, f2u(v), CTRL, 0xf, 0xf, true));
}
struct Params {
  const char *x, *wqkv, *wo, *Are, *Aim, *Bre, *Bim, *Cre, *Cim, *logdt, *dskip, *gatew;
  char *ws, *out;
};

DEV u32x2 s_gen(const Params& P, int bid, int gw, int lane, u32x4 (&kv)[4]) {
  char* ws = P.ws;
  const int d = 2 * bid + (gw >> 1), hw = gw & 1;
  const int pi = d * 128 + 64 * hw + lane;
  unsigned rdt, rds, rAre, rAim, rBre, rBim, rCre, rCim;
  u32x2 tag;
#define LDU16(dst, base, off) asm volatile("global_load_ushort %0, %1, %2" : "=v"(dst) : "v"(off), "s"(base) : "memory")
#define LD16(dst, base, off) asm volatile("global_load_dwordx4 %0, %1, %2" : "=v"(dst) : "v"(off), "s"(base) : "memory")
  asm volatile("global_load_dwordx2 %0, %1, %2 sc1" : "=v"(tag) : "v"(WS_EP), "s"(ws) : "memory");
  LDU16(rdt, P.logdt, d * 2); LDU16(rAre, P.Are, pi * 2); LDU16(rAim, P.Aim, pi * 2); LDU16(rBre, P.Bre, pi * 2);
  LDU16(rBim, P.Bim, pi * 2); LDU16(rCre, P.Cre, pi * 2); LDU16(rCim, P.Cim, pi * 2); LDU16(rds, P.dskip, d * 2);
  const int grow = 8 * bid + 2 * gw;
  u32x4 gx0, gx1, gwv;
  LD16(gx0, P.x, grow * 1024 + lane * 16); LD16(gx1, P.x, grow * 1024 + 1024 + lane * 16); LD16(gwv, P.gatew, lane * 16);
  PRIO(0);
  const unsigned reg = conv_region(gw >> 1) + hw * 17408, tab = B_TAB + (gw >> 1) * 5128;
  unsigned ea[16];
#pragma unroll
  for (int it = 0; it < 2; ++it)
#pragma unroll
    for (int e = 0; e < 8; e += 2) {
      const int l0 = 2047 - (1024 * hw + 512 * it + 8 * lane) - e;
      const unsigned pr = pack_bf16((float)l0, (float)(l0 - 1));
      ea[it * 8 + e] = tab + (l0 < 128 ? l0 : (int)(pr & 0xffffu) - 0x4280) * 8;
      ea[it * 8 + e + 1] = tab + (l0 - 1 < 128 ? l0 - 1 : (int)(pr >> 16) - 0x4280) * 8;
    }
#pragma unroll
  for (int i = 0; i < 16; ++i) asm volatile("" : "+v"(ea[i]));
  asm volatile("s_waitcnt vmcnt(3)" : "+v"(tag), "+v"(rdt), "+v"(rds), "+v"(rAre), "+v"(rAim), "+v"(rBre), "+v"(rBim), "+v"(rCre),
               "+v"(rCim)::"memory");
  if (gw == 0) {
    tag[0] += 1;
    *(volatile LDS u32x2*)(size_t)F_TAG = tag;
    lds_flag_set(F_TAGOK);
  }
  const float dt = round_bf16(expf(bf_lo(rdt)));
  const float a2 = (dt * bf_lo(rAre)) * 1.4426950408889634f;
  const float br = (dt * bf_lo(rAim)) * 0.15915494309189535f;
  const cf w1 = cseed(a2, br, 1.f), W = cseed(a2, br, 128.f);
  cf w8c = cseed(a2, br, 8.f); w8c.i = -w8c.i;
  cf R = cmul(cf{bf_lo(rCre), -bf_lo(rCim)}, cf{dt * bf_lo(rBre), -(dt * bf_lo(rBim))});
  unsigned rv[16], lv[41];
#define PK(v) pack_bf16((v).r, (v).i)
  cf p[8], Wp[17];
  p[0] = cf{1.f, 0.f}; p[1] = w1; Wp[1] = W;
  lv[0] = 0x3f80u; lv[1] = PK(w1); lv[8] = PK(W); rv[0] = PK(R);
#pragma unroll
  for (int i = 2; i < 8; ++i) { cmul2(p[i], p[i - 1], w1, R, R, w8c); lv[i] = PK(p[i]); rv[i - 1] = PK(R); }
#pragma unroll
  for (int i = 1; i < 8; ++i) { cf t; cmul2(t, W, p[i], R, R, w8c); lv[8 + i] = PK(t); rv[6 + i] = PK(R); }
  cmul2(Wp[2], W, W, R, R, w8c); rv[14] = PK(R);
  cmul2(Wp[3], Wp[2], W, R, R, w8c); rv[15] = PK(R);
  lv[16] = PK(Wp[2]); lv[20] = PK(Wp[3]);
#pragma unroll
  for (int i = 1; i < 4; ++i) { cf t, u; cmul2(t, Wp[2], p[2 * i], u, Wp[3], p[2 * i]); lv[16 + i] = PK(t); lv[20 + i] = PK(u); }
  cmul2(Wp[4], Wp[2], Wp[2], Wp[5], Wp[3], Wp[2]);
  cmul2(Wp[6], Wp[3], Wp[3], Wp[7], Wp[4], Wp[3]);
#pragma unroll
  for (int a = 0; a < 4; a += 2) {
    cf t, u; cmul2(t, Wp[4 + a], p[4], u, Wp[5 + a], p[4]);
    lv[24 + 2 * a] = PK(Wp[4 + a]); lv[25 + 2 * a] = PK(t); lv[26 + 2 * a] = PK(Wp[5 + a]); lv[27 + 2 * a] = PK(u);
  }
#pragma unroll
  for (int a = 8; a < 16; a += 2) { cmul2(Wp[a], Wp[a - 4], Wp[4], Wp[a + 1], Wp[a - 3], Wp[4]); lv[24 + a] = PK(Wp[a]); lv[25 + a] = PK(Wp[a + 1]); }
  Wp[16] = cmul(Wp[8], Wp[8]); lv[40] = PK(Wp[16]);
#undef PK
#pragma unroll
  for (int i = 0; i < 16; ++i) asm volatile("" : "+v"(rv[i]));
#pragma unroll
  for (int i = 1; i < 41; ++i) asm volatile("" : "+v"(lv[i]));
  asm volatile("s_waitcnt vmcnt(0)" : "+v"(gx0), "+v"(gx1), "+v"(gwv)::"memory");
  {
    float s0 = 0.f, s1 = 0.f;
#pragma unroll
    for (int j = 0; j < 4; ++j) {
      const float wl = bf_lo(gwv[j]), wh = bf_hi(gwv[j]);
      s0 = __builtin_fmaf(bf_lo(gx0[j]), wl, s0); s0 = __builtin_fmaf(bf_hi(gx0[j]), wh, s0);
      s1 = __builtin_fmaf(bf_lo(gx1[j]), wl, s1); s1 = __builtin_fmaf(bf_hi(gx1[j]), wh, s1);
    }
    auto sw = __builtin_amdgcn_permlane32_swap(f2u(s0), f2u(s1), false, false);
    float s = u2f(sw[0]) + u2f(sw[1]);
    auto sx = __builtin_amdgcn_permlane16_swap(f2u(s), f2u(s), false, false);
    s = u2f(sx[0]) + u2f(sx[1]);
    s = dpp_add<0x128>(s); s = dpp_add<0x124>(s); s = dpp_add<0x122>(s); s = dpp_add<0x121>(s);
    const float gv = round_bf16(__builtin_amdgcn_rcpf(1.0f + __expf(-round_bf16(s))));
    if ((lane & 31) == 0) st4_dev(ws, WS_G + (grow + (lane >> 5)) * 4, f2u(gv));
  }
  const int j = lane & 15, g = lane >> 4;
  unsigned t0a = tab + hw * 4 + (128 * (g >> 1) + 8 * j + 4 * (g & 1)) * 8;
  unsigned t1a = tab + hw * 4 + (g < 2 ? 256 + 64 * g + 4 * j : 384 + 64 * (g - 2) + 2 * j) * 8, t1b = t1a + (g < 2 ? 16 : 256);
  unsigned t2a = tab + hw * 4 + (512 + 64 * g + j) * 8;
  const float ds0 = (lane == 0 && hw == 0) ? bf_lo(rds) : 0.f;
  asm volatile("" : "+v"(t0a), "+v"(t1a), "+v"(t1b), "+v"(t2a));
  tag = get_tag();
  if (lane < 32) lds_w128(B_U + (gw >> 1) * 4608 + lane * 16, u32x4{0, 0, 0, 0});
  lds_wait<4, 1>(F_GD);
  PRIO(1);
  const unsigned wa = reg + lane * 4, ra = reg + (lane & 15) * 272 + (lane >> 4) * 16;
#pragma unroll
  for (int i = 0; i < 16; ++i) lds_w32(wa + i * 272, rv[i]);
#pragma unroll
  for (int i = 0; i < 41; ++i) lds_w32(wa + 4352 + i * 272, lv[i]);
  CBAR();
  f32x4 d0 = {0, 0, 0, 0}, d1 = d0, d2 = d0;
#pragma unroll
  for (int ks = 0; ks < 4; ++ks) {
    const u32x4 bfr = lds_r128(ra + ks * 64);
    d0 = mfma16(lds_r128(ra + 4352 + ks * 64), bfr, d0);
    d1 = mfma16(lds_r128(ra + 8704 + ks * 64), bfr, d1);
    d2 = mfma16(lds_r128(ra + 13056 + ks * 64), bfr, d2);
  }
  CBAR();
  d0[0] += ds0;
#define TW(a, v) *(LDS float*)(size_t)(a) = (v)
#pragma unroll
  for (int q = 0; q < 4; ++q) TW(t0a + 8 * q, d0[q]);
  TW(t1a, d1[0]); TW(t1a + 8, d1[1]); TW(t1b, d1[2]); TW(t1b + 8, d1[3]);
  if (lane <= 32) TW(t2a, d2[0]);
  if (g < 2) {
#pragma unroll
    for (int q = 1; q < 4; ++q) TW(t2a + 128 * q, d2[q]);
  }
#undef TW
  lds_flag_set(F_GP + gw * 4);
  lds_wait<1, 0>(F_GP + (gw ^ 1) * 4);
  f32x2 tp[19];
  float ta[19];
#pragma unroll
  for (int i = 0; i < 19; ++i) tp[i] = *(LDS f32x2*)(size_t)(i < 16 ? ea[i] : tab + (512 - (i - 16)) * 8);
  SB();
#pragma unroll
  for (int i = 0; i < 19; ++i) ta[i] = fadd1(tp[i][0], tp[i][1]);
#pragma unroll
  for (int it = 0; it < 2; ++it)
#pragma unroll
    for (int e = 0; e < 4; ++e) kv[it][e] = pack_bf16(ta[it * 8 + 2 * e], ta[it * 8 + 2 * e + 1]);
  {
    const float a = ta[16], b = ta[17], c = ta[18];
    const u32x4 nx = hw ? u32x4{0, 0, 0, 0} : u32x4{pack_bf16(a, a), pack_bf16(b, b), pack_bf16(b, c), pack_bf16(c, c)};
#pragma unroll
    for (int e = 0; e < 4; ++e) {
      kv[2][e] = __builtin_amdgcn_update_dpp(0u, lane == 0 ? kv[1][e] : kv[0][e], 0x134, 0xf, 0xf, false);
      kv[3][e] = __builtin_amdgcn_update_dpp(0u, lane == 0 ? nx[e] : kv[1][e], 0x134, 0xf, 0xf, false);
    }
  }
  asm volatile("" : "+v"(kv[0]), "+v"(kv[1]), "+v"(kv[2]), "+v"(kv[3])::"memory");
  return tag;
}

DEV float max3(float a, float b, float c) { return __builtin_fmaxf(__builtin_fmaxf(a, b), c); }
DEV float rowmax16(f32x16 s) {
  return max3(max3(max3(s[0], s[1], s[2]), max3(s[3], s[4], s[5]), max3(s[6], s[7], s[8])),
              max3(max3(s[9], s[10], s[11]), max3(s[12], s[13], s[14]), s[15]), s[15]);
}
DEV float amax3(float a, float b, float c) { float o; asm("v_max3_f32 %0, %1, %2, %3" : "=v"(o) : "v"(a), "v"(b), "v"(c)); return o; }
DEV float halfmax(float m) {
  auto r = __builtin_amdgcn_permlane32_swap(f2u(m), f2u(m), false, false);
  return __builtin_fmaxf(u2f(r[0]), u2f(r[1]));
}
DEV f32x8 lo8(f32x16 v) { return __builtin_shufflevector(v, v, 0, 1, 2, 3, 4, 5, 6, 7); }
DEV f32x8 hi8(f32x16 v) { return __builtin_shufflevector(v, v, 8, 9, 10, 11, 12, 13, 14, 15); }
#define EXPS(S, a, b) _Pragma("unroll") for (int i_ = a; i_ < b; ++i_) { float e_ = S[i_]; asm volatile("v_exp_f32 %0, %0" : "+v"(e_)); S[i_] = e_; }
#define PV(O, V, P8) asm volatile("v_smfmac_f32_32x32x32_bf16 %0, %1, %2, %3" : "+v"(O) : "v"(V), "v"(P8), "v"(idx))
#define QK(D, Q, NM) asm volatile("v_mfma_f32_32x32x16_bf16 %0, %1, %2, %3" : "=v"(D) : "v"(kn), "v"(Q), "v"(NM))
#define TILE(T, VL, VH, XL, XH)                                    \
  EXPS(SA, 0, 2); SB();                                            \
  PV(OB, XH, hi8(SBv)); SB();                                      \
  EXPS(SA, 2, 6); SB();                                            \
  QK(SBv, qB, nm); SB();                                          \
  kn = lds_r128(kb + ((T) + 1) * KVS); SB();                       \
  EXPS(SA, 6, 11); SB();                                           \
  PV(OA, VL, lo8(SA)); SB();                                       \
  EXPS(SA, 11, 16); SB();                                          \
  EXPS(SBv, 0, 2); SB();                                           \
  PV(OA, VH, hi8(SA)); SB();                                       \
  XL = lds_r128(vb0 + ((T) + 1) * KVS); SB();                      \
  EXPS(SBv, 2, 6); SB();                                           \
  QK(SA, qA, nm); SB();                                           \
  XH = lds_r128(vb1 + ((T) + 1) * KVS); SB();                      \
  EXPS(SBv, 6, 11); SB();                                          \
  PV(OB, VL, lo8(SBv)); SB();                                      \
  EXPS(SBv, 11, 16); SB();

DEV void attn_exact(const char* kp, int t0, int t1, u32x4 q, f32x16& O, float& m, int lane) {
  const int r = lane & 31, idx = (int)0xDDDDDDDDu;
  const f32x16 zero16 = {0, 0, 0, 0, 0, 0, 0, 0, 0, 0, 0, 0, 0, 0, 0, 0};
  const unsigned one = (r == 16 || r == 20) ? 0x3f803f80u : 0u, vo = (WS_VF - WS_KF) + (lane >> 5) * 256 + (r & 15) * 16;
  float mm = -__builtin_inff();
  O = zero16;
#pragma clang loop unroll(disable)
  for (int pass = 0; pass < 2; ++pass) {
#pragma clang loop unroll(disable)
    for (int t = t0; t < t1; ++t) {
      u32x4 k, v0, v1;
      asm volatile("global_load_dwordx4 %0, %3, %5 sc1\n\tglobal_load_dwordx4 %1, %4, %5 sc1\n\tglobal_load_dwordx4 %2, %4, %5 offset:512 sc1\n\t"
                   "s_waitcnt vmcnt(0)" : "=&v"(k), "=&v"(v0), "=&v"(v1) : "v"(t * 1024 + lane * 16), "v"(t * 1024 + vo), "s"(kp) : "memory");
      f32x16 s = mfma32(k, q, zero16);
      if (pass == 0) { mm = __builtin_fmaxf(mm, rowmax16(s)); continue; }
      if (r >= 16) v0 = v1 = u32x4{one, one, one, one};
#pragma unroll
      for (int i = 0; i < 16; ++i) s[i] = __builtin_amdgcn_exp2f(s[i] - mm);
      O = __builtin_amdgcn_smfmac_f32_32x32x32_bf16(as_frag(v0), __builtin_bit_cast(bf16x16, lo8(s)), O, idx, 0, 0);
      O = __builtin_amdgcn_smfmac_f32_32x32x32_bf16(as_frag(v1), __builtin_bit_cast(bf16x16, hi8(s)), O, idx, 0, 0);
    }
    if (pass == 0) mm = halfmax(mm);
  }
  m = mm;
}

DEV void b_attn(char* ws, int bid, int w, int lane) {
  const int head = 4 * (bid & 7) + ((bid >> 3) & 3), qt = (bid >> 5) * 8 + 2 * (w & 3), kh = w >> 2, t0 = kh * NKA;
  const int r = lane & 31, h = lane >> 5;
  const u32x2 tag = get_tag();
  unsigned kb = t0 * KVS + lane * 16;
  const unsigned oth = (r == 16 || r == 20) ? 2048 : 2064;
  unsigned vb0 = t0 * KVS + (r < 16 ? 1024 + h * 256 + r * 16 : oth);
  unsigned vb1 = t0 * KVS + (r < 16 ? 1536 + h * 256 + r * 16 : oth);
  int idx = (int)0xDDDDDDDDu;
  f32x16 OA, OB, SBv;
#pragma unroll
  for (int i = 0; i < 16; ++i) { OA[i] = 0.f; OB[i] = 0.f; SBv[i] = 0.f; }
  u32x4 xh = u32x4{0, 0, 0, 0};
  asm volatile("" : "+v"(SBv), "+v"(OA), "+v"(OB), "+v"(xh), "+v"(kb), "+v"(vb0), "+v"(vb1), "+v"(idx));
  if (w == 0) {
    lds_wait<4, 2>(F_GD);
    const unsigned v = (lane & 1) ? 0u : 0x3f803f80u;
    lds_w128(((lane >> 1) + (lane < 32 ? 0 : NKA - 16)) * KVS + 2048 + (lane & 1) * 16, u32x4{v, v, v, v});
    lds_flag_set(F_ONES);
  }
  if (w == 4) {
    const int cq = (head >> 1) / 3, ck = (16 + (head >> 1)) / 3, cv = (32 + (head >> 1)) / 3;
    const int g = lane >> 4, i = lane & 15;
    poll1(ws, WS_FA + (g == 0 ? ck * 16 + i : g == 1 ? cv * 16 + i : cq * 16 + 2 * (bid >> 5) + (i & 1)) * 128, tag);
    lds_wait<1, 0>(F_ONES);
    lds_flag_set(F_GO);
  } else {
    if (w < 4) lds_wait<4, 4>(F_PD);
    lds_wait<1, 0>(F_GO);
  }
  PRIO(2);
  u32x4 qA, qB, kn;
  const char* kp = ws + WS_KF + head * 65536;
  auto fetch = [&](int t) { dma16_dev(t * KVS, lane * 16, kp + t * 1024); dma16_dev(t * KVS + 1024, lane * 16, kp + (WS_VF - WS_KF) + t * 1024); };
  const int tw = t0 + (w & 3);
  asm volatile("global_load_dwordx4 %0, %3, %4 sc1\n\tglobal_load_dwordx4 %1, %3, %4 offset:1024 sc1\n\tglobal_load_dwordx4 %2, %3, %5 sc1"
               : "=&v"(qA), "=&v"(qB), "=&v"(kn) : "v"(lane * 16), "s"(ws + WS_QF + (head * 64 + qt) * 1024), "s"(kp + t0 * 1024) : "memory");
  fetch(tw); fetch(tw + 4);
  asm volatile("s_waitcnt vmcnt(4)" : "+v"(qA), "+v"(qB), "+v"(kn)::"memory");
  const f32x16 zero16 = {0, 0, 0, 0, 0, 0, 0, 0, 0, 0, 0, 0, 0, 0, 0, 0};
  f32x16 SA = mfma32(kn, qA, zero16);
  f32x16 sb0 = mfma32(kn, qB, zero16);
  SB();
  WAIT_VM(2);
  lds_flag_set(F_F0 + w * 4);
  asm volatile("s_nop 11" : "+v"(SA), "+v"(sb0));
  float mA = amax3(amax3(amax3(SA[0], SA[1], SA[2]), amax3(SA[3], SA[4], SA[5]), amax3(SA[6], SA[7], SA[8])),
                   amax3(amax3(SA[9], SA[10], SA[11]), amax3(SA[12], SA[13], SA[14]), amax3(SA[15], sb0[0], sb0[1])),
                   amax3(amax3(sb0[2], sb0[3], sb0[4]), amax3(sb0[5], sb0[6], sb0[7]), amax3(sb0[8], sb0[9], sb0[10])));
  mA = halfmax(amax3(mA, amax3(sb0[11], sb0[12], sb0[13]), amax3(sb0[14], sb0[15], sb0[15])));
  float mB = mA;
  f32x16 nm;
#pragma unroll
  for (int i = 0; i < 16; ++i) { nm[i] = -mA; SA[i] = fadd1(SA[i], nm[0]); }
  asm volatile("" : "+v"(nm), "+v"(SA));
  lds_wait<4, 0>(F_F0 + kh * 16);
  u32x4 vl = lds_r128(vb0), vh = lds_r128(vb1), xl;
  unsigned ev = 1u | (2u << 3) | (4u << 6) | (3u << 9) | (kh ? (5u << 15) | (7u << 18) : (5u << 12) | (6u << 21));
#pragma clang loop unroll(disable)
  for (int g = kh ? (64 - NKA) / 4 : NKA / 4; g > 0; --g) {
    asm volatile("" : "+s"(ev));
    TILE(0, vl, vh, xl, xh)
    TILE(1, xl, xh, vl, vh)
    TILE(2, vl, vh, xl, xh)
    const unsigned e = ev & 7;
    if (e) {
      if (e == 1) { WAIT_VM(0); lds_flag_set(F_F2 + w * 4); fetch(tw + 8); fetch(tw + 12); lds_wait<4, 0>(F_F2 + kh * 16); }
      else if (e == 2) { WAIT_VM(0); lds_flag_set(F_F1 + w * 4); lds_wait<4, 0>(F_F1 + kh * 16); }
      else if (e == 3) lds_wait<4, 0>(F_FS);
      else if (e == 4) PRIO(1);
      else if (e == 5) PRIO(0);
      else if (e == 6) lds_flag_set(F_AD + 16 + w * 4);
      else lds_wait<4, 0>(F_AD + 16);
    }
    TILE(3, xl, xh, vl, vh)
    kb += 4 * KVS; vb0 += 4 * KVS; vb1 += 4 * KVS;
    ev >>= 3;
  }
  asm volatile("s_nop 1");
  PV(OB, xh, hi8(SBv));
  asm volatile("s_nop 15" : "+v"(OA), "+v"(OB));
  if (__builtin_expect(__any(!(OA[8] <= 0x1p30f && OA[8] >= 0x1p-60f)), 0)) attn_exact(kp, t0, kh ? 64 : NKA, qA, OA, mA, lane);
  if (__builtin_expect(__any(!(OB[8] <= 0x1p30f && OB[8] >= 0x1p-60f)), 0)) attn_exact(kp, t0, kh ? 64 : NKA, qB, OB, mB, lane);
  if (kh) PRIO(3); else lds_wait<4, 0>(F_AD + 16);
  {
    const f32x16 X = kh ? OA : OB;
    const unsigned ex = B_EX + w * 2560 + lane * 16;
    *(LDS f32x4*)(size_t)ex = f32x4{X[0], X[1], X[2], X[3]};
    *(LDS f32x4*)(size_t)(ex + 1024) = f32x4{X[4], X[5], X[6], X[7]};
    *(LDS f32x2*)(size_t)(B_EX + w * 2560 + 2048 + lane * 8) = f32x2{X[8], kh ? mA : mB};
  }
  lds_flag_set(F_EX + w * 4);
  {
    const f32x16 X = kh ? OB : OA;
    const float m = kh ? mB : mA;
    f32x4 y0, y1;
    f32x2 y2;
#pragma clang loop unroll(disable)
    for (int n = 0; n < (1 << 21); ++n) {
      unsigned f;
      asm volatile("ds_read_b32 %0, %4\n\tds_read_b128 %1, %5\n\tds_read_b128 %2, %5 offset:1024\n\tds_read_b64 %3, %6\n\ts_waitcnt lgkmcnt(0)"
                   : "=&v"(f), "=&v"(y0), "=&v"(y1), "=&v"(y2)
                   : "v"(F_EX + (w ^ 4) * 4), "v"(B_EX + (w ^ 4) * 2560 + lane * 16), "v"(B_EX + (w ^ 4) * 2560 + 2048 + lane * 8) : "memory");
      if (__builtin_amdgcn_readfirstlane(f)) break;
      __builtin_amdgcn_s_sleep(1);
    }
    PRIO(3);
    const float M = __builtin_fmaxf(m, y2[1]);
    float sx = __builtin_amdgcn_exp2f(m - M), sy = __builtin_amdgcn_exp2f(y2[1] - M);
    const float inv = __builtin_amdgcn_rcpf(__builtin_fmaf(X[8], sx, y2[0] * sy));
    sx *= inv; sy *= inv;
    unsigned ov[4];
#pragma unroll
    for (int j = 0; j < 2; ++j) {
      ov[j] = pack_bf16(__builtin_fmaf(X[2 * j], sx, y0[2 * j] * sy), __builtin_fmaf(X[2 * j + 1], sx, y0[2 * j + 1] * sy));
      ov[2 + j] = pack_bf16(__builtin_fmaf(X[4 + 2 * j], sx, y1[2 * j] * sy), __builtin_fmaf(X[5 + 2 * j], sx, y1[2 * j + 1] * sy));
    }
    st16_dev(ws, WS_QF + (head * 64 + qt + kh) * 1024 + lane * 16, u32x4{ov[0], ov[1], ov[2], ov[3]});
  }
  publish(ws, WS_FO + ((qt + kh) * 32 + head) * 128, tag, lane);
  const int T = (bid & 7) + 8 * ((bid >> 3) & 7);
  poll1_atomic(ws, WS_FO + (T * 32 + 4 * w + (lane & 3)) * 128, tag, lane < 4);
#pragma unroll
  for (int i = 0; i < 4; ++i) dma16_dev(C_O + (4 * w + i) * 1024, lane * 16, ws + WS_QF + ((4 * w + i) * 64 + T) * 1024);
  WAIT_VM(0);
  lds_flag_set(F_OC + w * 4);
  if (bid == 0 && w == 0) {
    poll2(ws, WS_FY + lane * 128, WS_FY + (64 + lane) * 128, tag);
    poll2(ws, WS_FY + (128 + lane) * 128, WS_FY + (192 + lane) * 128, tag);
    if (lane == 0) st8_dev(ws, WS_EP, tag);
  }
}

#define MFMA16(D, A, B) asm volatile("v_mfma_f32_16x16x32_bf16 %0, %1, %2, %0" : "+v"(D) : "v"(A), "v"(B))
template <int CA, int CB>
DEV void conv_tiles(unsigned ka, char* ws, unsigned yo, unsigned ub) {
  f32x4 a0 = {0, 0, 0, 0}, a1 = a0, b0 = a0, b1 = a0;
  constexpr int NA = 8 * (CA + 2), NB = 8 * (CB + 2), LA = 4;
  u32x4 GA[8 + LA + 1], GB[8 + LA + 1], A[LA + 1];
  auto ga = [&](int q) { if (q < NA) GA[q % (8 + LA + 1)] = lds_r128(ub + 512 * (CA + 1) + 4096 - 64 * q); };
  auto gb = [&](int q) { if (q < NB) GB[q % (8 + LA + 1)] = lds_r128(ub + 512 * (CB + 1) + 4096 - 64 * q); };
  auto ra = [&](int q) { if (q < NB) A[q % (LA + 1)] = lds_r128(ka + 4032 - 64 * q); };
#pragma unroll
  for (int q = 0; q < LA; ++q) ra(q);
#pragma unroll
  for (int q = 0; q < 8 + LA; ++q) { gb(q); ga(q); }
  asm volatile("s_nop 1" : "+v"(a0), "+v"(a1), "+v"(b0), "+v"(b1));
#pragma unroll
  for (int s = 0; s < NB; ++s) {
    SB();
    MFMA16(b1, A[s % (LA + 1)], GB[s % (8 + LA + 1)]);
    SB();
    ra(s + LA);
    SB();
    if (s + 8 < NB) MFMA16(b0, A[s % (LA + 1)], GB[(s + 8) % (8 + LA + 1)]);
    SB();
    gb(s + 8 + LA);
    SB();
    if (s < NA) MFMA16(a1, A[s % (LA + 1)], GA[s % (8 + LA + 1)]);
    SB();
    ga(s + 8 + LA);
    SB();
    if (s + 8 < NA) MFMA16(a0, A[s % (LA + 1)], GA[(s + 8) % (8 + LA + 1)]);
  }
  asm volatile("s_nop 7" : "+v"(a0), "+v"(a1), "+v"(b0), "+v"(b1));
  st8_dev(ws, yo + 262144 * CA, u32x2{pack_bf16(a0[0], a0[1]), pack_bf16(a0[2], a0[3])});
  st8_dev(ws, yo + 262144 * (CA + 1), u32x2{pack_bf16(a1[0], a1[1]), pack_bf16(a1[2], a1[3])});
  st8_dev(ws, yo + 262144 * CB, u32x2{pack_bf16(b0[0], b0[1]), pack_bf16(b0[2], b0[3])});
  st8_dev(ws, yo + 262144 * (CB + 1), u32x2{pack_bf16(b1[0], b1[1]), pack_bf16(b1[2], b1[3])});
}

DEV void s_rest(const Params& P, u32x2 tag, int bid, int cw, int lane, const u32x4 (&kv)[4]) {
  char* ws = P.ws;
  const int head = 4 * (bid & 7) + ((bid >> 3) & 3);
  const int d = 2 * bid + (cw >> 1), hw = cw & 1;
  {
    PRIO(1);
    const unsigned ur = B_U + (cw >> 1) * 4608;
    const unsigned tb = conv_region(cw >> 1), ta = tb + (128 * hw + lane) * 16;
    unsigned Od[2][7];
#pragma unroll
    for (int it = 0; it < 2; ++it)
#pragma unroll
      for (int k = 0; k < 7; ++k) {
        Od[it][k] = __builtin_amdgcn_alignbit(k < 3 ? kv[it][k + 1] : kv[2 + it][k - 3], k < 4 ? kv[it][k] : kv[2 + it][k - 4], 16);
        asm volatile("" : "+v"(Od[it][k]));
      }
    if (hw == 1 && lane < 32) lds_w128(tb + (lane >> 2) * 4160 + 4096 + (lane & 3) * 16, u32x4{0, 0, 0, 0});
#pragma unroll
    for (int it = 0; it < 2; ++it) {
      const unsigned D[8] = {kv[it][0], kv[it][1], kv[it][2], kv[it][3], kv[2 + it][0], kv[2 + it][1], kv[2 + it][2], kv[2 + it][3]};
#pragma unroll
      for (int e = 0; e < 8; ++e) {
        const int k = e >> 1;
        lds_w128(ta + it * 1024 + e * 4160,
                 (e & 1) ? u32x4{Od[it][k], Od[it][k + 1], Od[it][k + 2], Od[it][k + 3]} : u32x4{D[k], D[k + 1], D[k + 2], D[k + 3]});
      }
    }
    lds_flag_set(F_T1 + cw * 4);
    lds_wait<1, 0>(F_XGO);
    lds_wait<1, 0>(F_T1 + (cw ^ 1) * 4);
    const int j = lane & 15, g = lane >> 4;
    unsigned ka = tb + ((7 - j) & 7) * 4160 + (2024 + 8 * g - 8 * (j >> 3)) * 2 - 4032;
    unsigned ub = ur + 512 - 32 + 32 * j + 16 * g - 4096;
    asm volatile("" : "+v"(ka), "+v"(ub));
    const unsigned yo = WS_XT + ((j >> 1) * 512 + d) * 64 + (16 * (j & 1) + 4 * g) * 2;
    if (hw) conv_tiles<2, 4>(ka, ws, yo, ub);
    else conv_tiles<0, 6>(ka, ws, yo, ub);
    lds_flag_set(F_CD + cw * 4);
    lds_wait<4, 0>(F_CD);
    lds_wait<8, 1>(F_F0);
    const char* kp = ws + WS_KF + head * 65536;
#pragma unroll
    for (int i = 0; i < 8; ++i) {
      const int n = 4 * i + cw, t = n < NKA - 16 ? 16 + n : 32 + n;
      dma16_dev(t * KVS, lane * 16, kp + t * 1024);
      dma16_dev(t * KVS + 1024, lane * 16, kp + (WS_VF - WS_KF) + t * 1024);
    }
    if (lane < 16) {
      const int n = 4 * (lane >> 1) + cw, t = n < NKA - 16 ? 16 + n : 32 + n;
      const unsigned v = (lane & 1) ? 0u : 0x3f803f80u;
      lds_w128(t * KVS + 2048 + (lane & 1) * 16, u32x4{v, v, v, v});
    }
    WAIT_VM(0);
    lds_flag_set(F_FS + cw * 4);
    PRIO(3);
    publish(ws, WS_FY + bid * 128 + cw * 8, tag, lane);
  }
  const int T = (bid & 7) + 8 * ((bid >> 3) & 7), dt = 4 * (bid >> 6) + cw;
  const int g = lane >> 4, q4 = (lane & 15) >> 2, pp = lane & 3;
  const unsigned st = C_ST + cw * 7424;
  const unsigned wa = st + (4 * (g >> 1) + q4) * 64 + (g & 1) * 32 + pp * 8;
  unsigned vw[4];
#pragma unroll
  for (int i = 0; i < 4; ++i) vw[i] = (lane >> 2) * 1024 + (lane & 3) * 16 + i * (16384 - 1024);
  u32x4 wf[28];
#pragma unroll
  for (int m = 0; m < 8; ++m) {
    dma16x4(st, vw, P.wo + dt * 64 + m * 65536);
    if (m == 7) break;
    WAIT_VM(0);
#pragma unroll
    for (int i = 0; i < 4; ++i) {
      const u32x2 lo = lds_tr(wa + i * 1024), hi = lds_tr(wa + i * 1024 + 512);
      wf[4 * m + i] = u32x4{lo[0], lo[1], hi[0], hi[1]};
    }
    asm volatile("s_waitcnt lgkmcnt(0)" : "+v"(wf[4 * m]), "+v"(wf[4 * m + 1]), "+v"(wf[4 * m + 2]), "+v"(wf[4 * m + 3])::"memory");
  }
  const int tl = lane & 31, h = lane >> 5, t = 32 * T + tl;
  poll2(ws, WS_FY + (16 * dt + (lane >> 2)) * 128 + (lane & 3) * 8, WS_FY + (4 * T + ((lane >> 2) & 3)) * 128 + (lane & 3) * 8, tag);
  dma16_dev(st + 4096, lane * 16, ws + WS_XT + (T * 512 + 32 * dt) * 64);
  dma16_dev(st + 5120, lane * 16, ws + WS_XT + (T * 512 + 32 * dt) * 64 + 1024);
  float gt;
  asm volatile("global_load_dword %0, %1, %2 sc1\n\ts_waitcnt vmcnt(0)" : "=&v"(gt) : "v"(t * 4), "s"(ws + WS_G) : "memory");
  f32x16 acc;
#pragma unroll
  for (int i = 0; i < 16; ++i) acc[i] = 0.f;
  asm volatile("" : "+v"(acc));
  const unsigned oa = C_O + lane * 16;
  PRIO(0); lds_wait<1, 8>(F_EX + 28); PRIO(3);
  unsigned have = 0;
  const unsigned oca = F_OC + (lane & 7) * 4;
  auto need = [&](int m) {
#pragma clang loop unroll(disable)
    for (int n = 0; !(have & (1u << m)) && n < (1 << 21); ++n) {
      unsigned f;
      asm volatile("ds_read_b32 %0, %1\n\ts_waitcnt lgkmcnt(0)" : "=&v"(f) : "v"(oca) : "memory");
      have = (unsigned)__ballot(f != 0);
    }
    CBAR();
  };
  need(0);
  u32x4 of[3], wl[2];
  u32x2 y[4];
  float yb[16];
  const float omg = 1.0f - gt;
  of[0] = lds_r128(oa); of[1] = lds_r128(oa + 1024);
#pragma unroll
  for (int m = 0; m < 4; ++m) y[m] = lds_tr(st + 4096 + (8 * m + 4 * (g >> 1) + q4) * 64 + (g & 1) * 32 + pp * 8);
#pragma unroll
  for (int k = 0; k < 32; ++k) {
    SB();
    if (k < 28) MFMA32(acc, wf[k], of[k % 3]); else MFMA32(acc, wl[k & 1], of[k % 3]);
    SB();
    if ((k & 3) == 2 && k < 30) need((k >> 2) + 1);
    if (k < 30) of[(k + 2) % 3] = lds_r128(oa + (k + 2) * 1024);
    if (k >= 26 && k < 30) {
      const u32x2 lo = lds_tr(wa + (k - 26) * 1024), hi = lds_tr(wa + (k - 26) * 1024 + 512);
      wl[k & 1] = u32x4{lo[0], lo[1], hi[0], hi[1]};
    }
    if (k >= 8 && k < 16) {
      yb[2 * (k - 8)] = omg * bf_lo(y[(k - 8) >> 1][k & 1]); yb[2 * (k - 8) + 1] = omg * bf_hi(y[(k - 8) >> 1][k & 1]);
      asm volatile("" : "+v"(yb[2 * (k - 8)]), "+v"(yb[2 * (k - 8) + 1]));
    }
  }
  asm volatile("s_nop 15" : "+v"(acc));
#pragma unroll
  for (int m = 0; m < 4; m += 2) {
    u32x2 a, b;
#pragma unroll
    for (int e = 0; e < 2; ++e) {
      a[e] = pack_bf16(__builtin_fmaf(gt, acc[4 * m + 2 * e], yb[4 * m + 2 * e]), __builtin_fmaf(gt, acc[4 * m + 2 * e + 1], yb[4 * m + 2 * e + 1]));
      b[e] = pack_bf16(__builtin_fmaf(gt, acc[4 * m + 4 + 2 * e], yb[4 * m + 4 + 2 * e]), __builtin_fmaf(gt, acc[4 * m + 5 + 2 * e], yb[4 * m + 5 + 2 * e]));
    }
    auto s0 = __builtin_amdgcn_permlane32_swap(a[0], b[0], false, false);
    auto s1 = __builtin_amdgcn_permlane32_swap(a[1], b[1], false, false);
    const unsigned r0 = s0[0], r1 = s1[0], r2 = s0[1], r3 = s1[1];
    *(u32x4*)(P.out + (size_t)(t * 1024 + (32 * dt + 8 * m + 8 * h) * 2)) = u32x4{r0, r1, r2, r3};
  }
}

__global__ __launch_bounds__(768, 3) void gated_mix_kernel(Params P) {
  const int bid = blockIdx.x, lane = threadIdx.x & 63;
  const int w = __builtin_amdgcn_readfirstlane(threadIdx.x >> 6);
  if (threadIdx.x < 32) asm volatile("ds_write_b128 %0, %1\n\ts_waitcnt lgkmcnt(0)" ::"v"(FL + lane * 16), "v"(u32x4{0, 0, 0, 0}) : "memory");
  __builtin_amdgcn_s_barrier();
  if (__builtin_expect(w < 8, 1)) {
    const int xcd = bid & 7, j = bid >> 3;
    const int tb = 4 * (xcd >> 1) + (j & 3), c = 8 * (xcd & 1) + (j >> 2);
    const int s0 = (2 * (xcd >> 1) + 4 * (xcd & 1)) & 7;
    if (__builtin_expect(w < 4, 1)) {
      a_load(P.x, P.wqkv, P.ws, bid, tb, c, s0, w, lane);
    } else {
      PRIO(3);
      if (c < 10) a_gemm<0>(P.ws, tb, c, w - 4, lane);
      else if (c == 10) a_gemm<1>(P.ws, tb, c, w - 4, lane);
      else a_gemm<3>(P.ws, tb, c, w - 4, lane);
    }
    b_attn(P.ws, bid, w, lane);
  } else {
    PRIO(3);
    u32x4 kv[4];
    const u32x2 tag = s_gen(P, bid, w - 8, lane, kv);
    s_rest(P, tag, bid, w - 8, lane, kv);
  }
}

extern "C" void kernel_launch(void* const* d_in, const int* in_sizes, int n_in, void* d_out, int out_size, void* d_ws,
                              size_t ws_size, hipStream_t stream) {
  (void)in_sizes; (void)n_in; (void)out_size; (void)ws_size;
  (void)hipFuncSetAttribute((const void*)gated_mix_kernel, hipFuncAttributeMaxDynamicSharedMemorySize, LDS_BYTES);
  Params P;
  P.x = (const char*)d_in[0]; P.wqkv = (const char*)d_in[1]; P.wo = (const char*)d_in[2];
  P.Are = (const char*)d_in[4]; P.Aim = (const char*)d_in[5]; P.Bre = (const char*)d_in[6]; P.Bim = (const char*)d_in[7];
  P.Cre = (const char*)d_in[8]; P.Cim = (const char*)d_in[9]; P.logdt = (const char*)d_in[10]; P.dskip = (const char*)d_in[11];
  P.gatew = (const char*)d_in[12]; P.ws = (char*)d_ws; P.out = (char*)d_out;
  gated_mix_kernel<<<256, 768, LDS_BYTES, stream>>>(P);
}
```
